```python
import torch

def _softmax(x, axis=-1):
    return torch.softmax(x, dim=axis)
def _gelu(x):
    return torch.nn.functional.gelu(x, approximate="tanh")
def _silu(x):
    return torch.nn.functional.silu(x)
def _sigmoid(x):
    return torch.sigmoid(x)
def _layernorm(x, axis=-1, eps=1e-5):
    m = x.mean(dim=axis, keepdim=True)
    v = x.var(dim=axis, keepdim=True, unbiased=False)
    return (x - m) / torch.sqrt(v + eps)
def _scatter_add(tgt, idx, src, axis=0):
    return tgt.clone().index_add_(axis, idx, src)
def _segment_sum(src, seg, n):
    out = torch.zeros((n,) + src.shape[1:], dtype=src.dtype)
    return out.index_add_(0, seg, src)
def _pad(x, pw):
    flat = tuple(v for pair in reversed(pw) for v in pair)
    return torch.nn.functional.pad(x, flat)

kernel_name = 'concat_heads__grouped_kv_mqa__softmax_full_mha__prompt'

def setup_inputs():

    _w = (lambda a: (lambda t: t.to(torch.bfloat16) if t.is_floating_point() else t)(torch.as_tensor(a)))
    def _A_setup():
        B, S, D, H = 1, 2048, 1024, 32
        Dh = D // H
        return {
            "x": _w((lambda *s: torch.randn(*s, dtype=torch.bfloat16) * 0.05)(B, S, D)),
            "wq": _w((lambda *s: torch.randn(*s, dtype=torch.bfloat16) * 0.8)(D, H * Dh)),
            "wk": _w((lambda *s: torch.randn(*s, dtype=torch.bfloat16) * 0.8)(D, Dh)),
            "wv": _w((lambda *s: torch.randn(*s, dtype=torch.bfloat16) * 0.1)(D, Dh)),
            "wo": _w((lambda *s: torch.randn(*s, dtype=torch.bfloat16) * 0.1)(H * Dh, D)),
        }
    _A_in = _A_setup()

    def _B_setup():
        B, S, D = 1, 2048, 1024
        return {
            "x": _w((lambda *s: torch.randn(*s, dtype=torch.bfloat16) * 0.2)(B, S, D)),
            "wqkv": _w((lambda *s: torch.randn(*s, dtype=torch.bfloat16) * 0.2)(D, 3 * D)),
            "wo": _w((lambda *s: torch.randn(*s, dtype=torch.bfloat16) * 0.05)(D, D)),
        }
    _B_in = _B_setup()

    return {
        "A_x": _A_in["x"],
        "A_wq": _A_in["wq"],
        "A_wk": _A_in["wk"],
        "A_wv": _A_in["wv"],
        "A_wo": _A_in["wo"],
        "B_x": _B_in["x"],
        "B_wqkv": _B_in["wqkv"],
        "B_wo": _B_in["wo"],
    }

def reference(A_x, A_wq, A_wk, A_wv, A_wo, B_x, B_wqkv, B_wo):
    def _A_ref(x, wq, wk, wv, wo):
        B, S, D = x.shape
        H = 32
        Dh = D // H
        q = (lambda x, *d: x.permute(*d))((x @ wq).reshape(B, S, H, Dh), 0, 2, 1, 3)
        k = x @ wk
        v = x @ wv
        scores = torch.einsum("bhqd,bkd->bhqk", q, k) * (Dh ** -0.5)
        p = _softmax(scores, axis=-1)
        o = torch.einsum("bhqk,bkd->bhqd", p, v)
        return (lambda x, *d: x.permute(*d))(o, 0, 2, 1, 3).reshape(B, S, H * Dh) @ wo

    def _B_ref(x, wqkv, wo):
        B, S, D = x.shape
        H = 32; Dh = D // H
        qkv = (lambda x, *d: x.permute(*d))((x @ wqkv).reshape(B, S, 3, H, Dh), 2, 0, 3, 1, 4)
        q, k, v = qkv[0], qkv[1], qkv[2]
        scale = Dh ** -0.5
        scores = torch.einsum("bhqd,bhkd->bhqk", q, k) * scale
        p = _softmax(scores, axis=-1)
        o = torch.einsum("bhqk,bhkd->bhqd", p, v)
        return (lambda x, *d: x.permute(*d))(o, 0, 2, 1, 3).reshape(B, S, D) @ wo

    a = _A_ref(A_x, A_wq, A_wk, A_wv, A_wo)
    b = _B_ref(B_x, B_wqkv, B_wo)
    return torch.concatenate([a, b], axis=-1)
```

```cpp
#include <hip/hip_runtime.h>
#include <hip/hip_bf16.h>
#include <stdint.h>

typedef __attribute__((ext_vector_type(8))) __bf16 bf16x8;
typedef __attribute__((ext_vector_type(2))) __bf16 bf16x2;
typedef __attribute__((ext_vector_type(16))) float f32x16;
typedef __attribute__((ext_vector_type(4))) float f32x4;
typedef __attribute__((ext_vector_type(2))) float f32x2;
typedef __attribute__((ext_vector_type(4))) unsigned u32x4;
typedef __attribute__((ext_vector_type(2))) unsigned u32x2;
typedef __attribute__((ext_vector_type(4))) short s16x4;
typedef __attribute__((ext_vector_type(8))) short s16x8;
typedef __attribute__((address_space(3))) const char* lds_cptr;
typedef __attribute__((address_space(3))) char* lds_ptr;

#define DI __device__ __forceinline__
#define MFMA32(a, b, c) __builtin_amdgcn_mfma_f32_32x32x16_bf16((a), (b), (c), 0, 0, 0)
#define MFMA16(a, b, c) __builtin_amdgcn_mfma_f32_16x16x32_bf16((a), (b), (c), 0, 0, 0)

constexpr int S_TOK = 2048, DM = 1024;
constexpr size_t WS_QF = 0;
constexpr size_t WS_KV = WS_QF + (size_t)64 * 131072;
constexpr size_t WS_OF = WS_KV + (size_t)33 * 262144;
constexpr float QSCALE = 0.17677669529663687f * 1.4426950408889634f;

DI unsigned cvtpk(float lo, float hi) { f32x2 v = {lo, hi}; bf16x2 b = __builtin_convertvector(v, bf16x2); return __builtin_bit_cast(unsigned, b); }
DI bf16x8 pack8(const f32x16& a, int o) {
    u32x4 w = {cvtpk(a[o], a[o + 1]), cvtpk(a[o + 2], a[o + 3]), cvtpk(a[o + 4], a[o + 5]), cvtpk(a[o + 6], a[o + 7])};
    return __builtin_bit_cast(bf16x8, w);
}
DI bf16x8 lds_rd128(lds_cptr p) { return *(const __attribute__((address_space(3))) bf16x8*)p; }
DI s16x4 lds_tr(lds_cptr p) { return __builtin_amdgcn_ds_read_tr16_b64_v4i16((__attribute__((address_space(3))) s16x4*)p); }
DI bf16x8 cat8(s16x4 lo, s16x4 hi) { s16x8 r = __builtin_shufflevector(lo, hi, 0, 1, 2, 3, 4, 5, 6, 7); return __builtin_bit_cast(bf16x8, r); }

template <int OFF> DI void glds16(unsigned voff, const void* sbase, unsigned ldsdst) {
    unsigned keep;
    asm volatile("s_mov_b32 %0, m0\n\ts_mov_b32 m0, %3\n\ts_nop 0\n\tglobal_load_lds_dwordx4 %1, %2 offset:%c4\n\ts_mov_b32 m0, %0"
                 : "=&s"(keep) : "v"(voff), "s"(sbase), "s"(ldsdst), "i"(OFF) : "memory");
}
DI void glds4(unsigned voff, const void* sbase, unsigned ldsdst) {
    unsigned keep;
    asm volatile("s_mov_b32 %0, m0\n\ts_mov_b32 m0, %3\n\ts_nop 0\n\tglobal_load_lds_dword %1, %2\n\ts_mov_b32 m0, %0"
                 : "=&s"(keep) : "v"(voff), "s"(sbase), "s"(ldsdst) : "memory");
}
#define WAIT_BAR(N) asm volatile("s_waitcnt vmcnt(" #N ") lgkmcnt(0)\n\ts_barrier" ::: "memory")

constexpr int P1_X = 0, P1_W = 16384, P1_S = 49152, P1_SLOT = 51200, P1_LDS = 3 * P1_SLOT;

template <int MODE, bool STRIP>
DI void proj_tile(char* smem, const char* xg, const char* wg, int ldw  , const char* sg  , char* dst  ,
                  char* kvA  , int ct) {
    const int tid = threadIdx.x, lane = tid & 63, w = __builtin_amdgcn_readfirstlane(tid >> 6);
    const int wm = w >> 2, wn = w & 3, r = lane & 31, h = lane >> 5;
    const unsigned lds0 = (unsigned)(uintptr_t)smem;
    const unsigned xo0 = (unsigned)((16 * w + (lane >> 3)) * 2048 + (((lane & 7) ^ (lane >> 4)) << 4));
    const unsigned xo1 = (unsigned)((16 * w + 8 + (lane >> 3)) * 2048 + (((lane & 7) ^ (4 + (lane >> 4))) << 4));
    unsigned wo[4];
#pragma unroll
    for (int u = 0; u < 4; ++u) wo[u] = (unsigned)((8 * w + 2 * u + (lane >> 5)) * ldw + (((lane & 31) ^ ((2 * (u & 1) + (lane >> 5)) << 2)) << 4));
    const unsigned so = (unsigned)((8 * w + (lane >> 3)) * 64 + (lane & 7) * 4);
    auto stage = [&](int t, int slot) {
        const unsigned l = lds0 + slot * P1_SLOT;
        const char* xs = xg + t * 128; const char* wsrc = wg + (size_t)t * 64 * ldw;
        glds16<0>(xo0, xs, l + P1_X + w * 2048); glds16<0>(xo1, xs, l + P1_X + w * 2048 + 1024);
#pragma unroll
        for (int u = 0; u < 4; ++u) glds16<0>(wo[u], wsrc, l + P1_W + w * 4096 + u * 1024);
        if (STRIP) glds4(so, sg + t * 4096, l + P1_S + w * 256);
    };
    const int y = h ^ ((r >> 1) & 7);
    lds_cptr xb[4];
#pragma unroll
    for (int s = 0; s < 4; ++s) xb[s] = (lds_cptr)smem + P1_X + (64 * wm + r) * 128 + (((2 * s) ^ y) << 4);
    const int q4 = (lane & 15) >> 2, p4 = lane & 3, blk = (lane >> 4) & 1;
    lds_cptr wb[2];
#pragma unroll
    for (int j = 0; j < 2; ++j) wb[j] = (lds_cptr)smem + P1_W + (8 * h + q4) * 512 + (((2 * wn + j) ^ q4) << 6) + blk * 32 + p4 * 8;
    const int g16 = lane >> 4, c16 = lane & 15;
    lds_cptr sxb[2];
#pragma unroll
    for (int ks = 0; ks < 2; ++ks) sxb[ks] = (lds_cptr)smem + P1_X + (16 * w + c16) * 128 + (((4 * ks + g16) ^ ((c16 >> 1) & 7)) << 4);
    const lds_cptr swb = (lds_cptr)smem + P1_S + (8 * g16 + q4) * 32 + p4 * 8;

    f32x16 acc[2][2];
#pragma unroll
    for (int i = 0; i < 2; ++i)
#pragma unroll
        for (int j = 0; j < 2; ++j) acc[i][j] = f32x16{};
    f32x4 sacc = {0.f, 0.f, 0.f, 0.f};

    stage(0, 0); stage(1, 1);
    int slot = 0;
    for (int t = 0; t < 16; ++t) {
        if (t < 15) { if (STRIP) WAIT_BAR(7); else WAIT_BAR(6); } else WAIT_BAR(0);
        if (t + 2 < 16) stage(t + 2, slot == 0 ? 2 : slot - 1);
        const int so_ = slot * P1_SLOT;
#pragma unroll
        for (int s = 0; s < 4; ++s) {
            bf16x8 xf[2], wf[2];
#pragma unroll
            for (int i = 0; i < 2; ++i) xf[i] = lds_rd128(xb[s] + so_ + i * 4096);
#pragma unroll
            for (int j = 0; j < 2; ++j) wf[j] = cat8(lds_tr(wb[j] + so_ + (16 * s) * 512), lds_tr(wb[j] + so_ + (16 * s + 4) * 512));
#pragma unroll
            for (int i = 0; i < 2; ++i)
#pragma unroll
                for (int j = 0; j < 2; ++j) acc[i][j] = (MODE == 2) ? MFMA32(xf[i], wf[j], acc[i][j]) : MFMA32(wf[j], xf[i], acc[i][j]);
        }
        if (STRIP) {
#pragma unroll
            for (int ks = 0; ks < 2; ++ks) {
                const bf16x8 xf = lds_rd128(sxb[ks] + so_);
                const bf16x8 wf = cat8(lds_tr(swb + so_ + (32 * ks) * 32), lds_tr(swb + so_ + (32 * ks + 4) * 32));
                sacc = (ct < 2) ? MFMA16(wf, xf, sacc) : MFMA16(xf, wf, sacc);
            }
        }
        slot = slot == 2 ? 0 : slot + 1;
    }
    constexpr int HSTR = (MODE == 0) ? 131072 : 262144, TSTR = (MODE == 0) ? 2048 : 4096, FOFF = (MODE == 2) ? 2048 : 0;
#pragma unroll
    for (int i = 0; i < 2; ++i)
#pragma unroll
        for (int j = 0; j < 2; ++j) {
            f32x16 a = acc[i][j];
            if (MODE == 0) a = a * QSCALE;
            char* d = dst + (size_t)(2 * wn + j) * HSTR + (2 * wm + i) * TSTR + FOFF + lane * 16;
            *(bf16x8*)d = pack8(a, 0);
            *(bf16x8*)(d + 1024) = pack8(a, 8);
        }
    if (STRIP) {
        const u32x2 v = {cvtpk(sacc[0], sacc[1]), cvtpk(sacc[2], sacc[3])};
        const int hh = g16 & 1, jj = g16 >> 1;
        char* d = kvA + (w >> 1) * 4096;
        if (ct < 2) d += ct * 1024 + (16 * (w & 1) + c16 + 32 * hh) * 16 + jj * 8;
        else        d += 2048 + (w & 1) * 1024 + (16 * (ct & 1) + c16 + 32 * hh) * 16 + jj * 8;
        *(u32x2*)d = v;
    }
}

__global__ __launch_bounds__(512) void proj_kernel(const char* __restrict__ xA, const char* __restrict__ wq, const char* __restrict__ wk, const char* __restrict__ wv,
                                                   const char* __restrict__ xB, const char* __restrict__ wqkv, char* __restrict__ ws) {
    extern __shared__ __attribute__((aligned(16))) char smem[];
    const int b = blockIdx.x, xcd = b & 7, idx = b >> 3, cg = xcd & 3, rb = (xcd >> 2) * 8 + (idx & 7), ct = idx >> 3;
    if (cg == 0) {
        const char* sg = (ct < 2 ? wk : wv) + (ct & 1) * 32;
        proj_tile<0, true>(smem, xA + (size_t)rb * 128 * 2048, wq + ct * 512, 2048, sg, ws + WS_QF + (size_t)(ct * 8) * 131072 + rb * 4 * 2048,
                           ws + WS_KV + (size_t)32 * 262144 + rb * 4 * 4096, ct);
    } else {
        const char* xg = xB + (size_t)rb * 128 * 2048; const char* wg = wqkv + (cg - 1) * 2048 + ct * 512;
        if (cg == 1)      proj_tile<0, false>(smem, xg, wg, 6144, nullptr, ws + WS_QF + (size_t)(32 + ct * 8) * 131072 + rb * 4 * 2048, nullptr, ct);
        else if (cg == 2) proj_tile<1, false>(smem, xg, wg, 6144, nullptr, ws + WS_KV + (size_t)(ct * 8) * 262144 + rb * 4 * 4096, nullptr, ct);
        else              proj_tile<2, false>(smem, xg, wg, 6144, nullptr, ws + WS_KV + (size_t)(ct * 8) * 262144 + rb * 4 * 4096, nullptr, ct);
    }
}

constexpr int AT_NW = 4, AT_QT = 2, AT_SLOT = 16384, AT_LDS = 3 * AT_SLOT;

DI float half_max(float m) {
    auto rr = __builtin_amdgcn_permlane32_swap(__float_as_uint(m), __float_as_uint(m), false, false);
    return fmaxf(__uint_as_float(rr[0]), __uint_as_float(rr[1]));
}
DI float max16(const f32x16& s) {
    float a = fmaxf(fmaxf(s[0], s[1]), s[2]), b = fmaxf(fmaxf(s[3], s[4]), s[5]);
#pragma unroll
    for (int i = 6; i < 14; i += 4) { a = fmaxf(fmaxf(a, s[i]), s[i + 1]); b = fmaxf(fmaxf(b, s[i + 2]), s[i + 3]); }
    return fmaxf(fmaxf(a, s[14]), fmaxf(b, s[15]));
}

DI void attn_tile_safe(const char* qp, const char* kv, char* op, int lane) {
    const bf16x8 q0 = *(const bf16x8*)(qp + lane * 16), q1 = *(const bf16x8*)(qp + 1024 + lane * 16);
    f32x16 o = f32x16{}; float m = -INFINITY, l = 0.f;
    for (int kb = 0; kb < 64; ++kb) {
        const char* p = kv + kb * 4096 + lane * 16;
        const bf16x8 k0 = *(const bf16x8*)p, k1 = *(const bf16x8*)(p + 1024), v0 = *(const bf16x8*)(p + 2048), v1 = *(const bf16x8*)(p + 3072);
        f32x16 s = MFMA32(k0, q0, f32x16{}); s = MFMA32(k1, q1, s);
        const float mn = fmaxf(m, half_max(max16(s)));
        const float alpha = __builtin_amdgcn_exp2f(m - mn); m = mn;
        float ls = 0.f;
#pragma unroll
        for (int i = 0; i < 16; ++i) s[i] = __builtin_amdgcn_exp2f(s[i] - mn);
        const bf16x8 p0 = pack8(s, 0), p1 = pack8(s, 8);
#pragma unroll
        for (int i = 0; i < 8; ++i) ls += (float)p0[i] + (float)p1[i];
        l = l * alpha + ls;
        o = o * alpha;
        o = MFMA32(v0, p0, o); o = MFMA32(v1, p1, o);
    }
    auto rr = __builtin_amdgcn_permlane32_swap(__float_as_uint(l), __float_as_uint(l), false, false);
    const float inv = 1.0f / (__uint_as_float(rr[0]) + __uint_as_float(rr[1]));
    o = o * inv;
    *(bf16x8*)(op + lane * 16) = pack8(o, 0);
    *(bf16x8*)(op + 1024 + lane * 16) = pack8(o, 8);
}

__global__ __launch_bounds__(AT_NW * 64, 2) void attn_kernel(char* __restrict__ ws) {
    extern __shared__ __attribute__((aligned(16))) char smem[];
    const int tid = threadIdx.x, lane = tid & 63, w = __builtin_amdgcn_readfirstlane(tid >> 6);
    constexpr int WG_PER_HEAD = 64 / (AT_NW * AT_QT), NWG = 64 * WG_PER_HEAD;
    const int b = blockIdx.x, xcd = b & 7, idx = b >> 3;
    const int hs = xcd * 8 + idx / WG_PER_HEAD;
    const int tb0 = (idx % WG_PER_HEAD) * (AT_NW * AT_QT) + w * AT_QT;
    const char* kv = ws + WS_KV + (size_t)(hs < 32 ? 32 : hs - 32) * 262144;
    const char* qp = ws + WS_QF + (size_t)hs * 131072 + tb0 * 2048;
    char* op = ws + WS_OF + (size_t)(hs >> 5) * 4194304 + (size_t)tb0 * 65536 + (hs & 31) * 2048;
    const unsigned lds0 = (unsigned)(uintptr_t)smem;
    constexpr int PIECES = 16 / AT_NW;
    const unsigned voff = (unsigned)(lane * 16 + w * PIECES * 1024);
    auto stage = [&](int t, int slot) {
        const char* src = kv + t * AT_SLOT; const unsigned l = lds0 + slot * AT_SLOT + w * PIECES * 1024;
        glds16<0>(voff, src, l);
        if (PIECES > 1) glds16<1024>(voff, src, l);
        if (PIECES > 2) { glds16<2048>(voff, src, l); glds16<3072>(voff, src, l); }
    };
    stage(0, 0); stage(1, 1);
    bf16x8 q[AT_QT][2];
#pragma unroll
    for (int t = 0; t < AT_QT; ++t) { q[t][0] = *(const bf16x8*)(qp + t * 2048 + lane * 16); q[t][1] = *(const bf16x8*)(qp + t * 2048 + 1024 + lane * 16); }
    const unsigned onev = ((((lane & 15) >> 2) & 1) == ((lane >> 4) & 1)) ? 0x3f803f80u : 0u;
    const bf16x8 ones = __builtin_bit_cast(bf16x8, (u32x4){onev, onev, onev, onev});
    f32x16 o[AT_QT], negm[AT_QT]; f32x4 l[AT_QT];
#pragma unroll
    for (int t = 0; t < AT_QT; ++t) { o[t] = f32x16{}; l[t] = f32x4{}; }
    const lds_cptr kb0 = (lds_cptr)smem + lane * 16;

    asm volatile("s_waitcnt vmcnt(0)\n\ts_barrier" ::: "memory");
    {
        const bf16x8 k0 = lds_rd128(kb0), k1 = lds_rd128(kb0 + 1024);
#pragma unroll
        for (int t = 0; t < AT_QT; ++t) {
            f32x16 s = MFMA32(k0, q[t][0], f32x16{}); s = MFMA32(k1, q[t][1], s);
            const float nm = -half_max(max16(s));
#pragma unroll
            for (int i = 0; i < 16; ++i) negm[t][i] = nm;
        }
    }
    int slot = 0;
    for (int st = 0; st < 16; ++st) {
        if (st > 0) { if (st < 15) { if (PIECES == 4) WAIT_BAR(4); else if (PIECES == 2) WAIT_BAR(2); else WAIT_BAR(1); } else WAIT_BAR(0); }
        if (st + 2 < 16) stage(st + 2, slot == 0 ? 2 : slot - 1);
        const lds_cptr kp = kb0 + slot * AT_SLOT;
#pragma unroll
        for (int kb = 0; kb < 4; ++kb) {
            const bf16x8 k0 = lds_rd128(kp + kb * 4096), k1 = lds_rd128(kp + kb * 4096 + 1024);
            const bf16x8 v0 = lds_rd128(kp + kb * 4096 + 2048), v1 = lds_rd128(kp + kb * 4096 + 3072);
#pragma unroll
            for (int t = 0; t < AT_QT; ++t) {
                f32x16 s = MFMA32(k0, q[t][0], negm[t]); s = MFMA32(k1, q[t][1], s);
#pragma unroll
                for (int i = 0; i < 16; ++i) s[i] = __builtin_amdgcn_exp2f(s[i]);
                const bf16x8 p0 = pack8(s, 0), p1 = pack8(s, 8);
                o[t] = MFMA32(v0, p0, o[t]); o[t] = MFMA32(v1, p1, o[t]);
                l[t] = MFMA16(ones, p0, l[t]); l[t] = MFMA16(ones, p1, l[t]);
            }
        }
        slot = slot == 2 ? 0 : slot + 1;
    }
    unsigned redo = 0;
#pragma unroll
    for (int t = 0; t < AT_QT; ++t) {
        const float lsum = l[t][0];
        float omax = 0.f;
#pragma unroll
        for (int i = 0; i < 16; ++i) omax = fmaxf(omax, fabsf(o[t][i]));
        const bool ok = (lsum < 0x1p100f) && (omax < 0x1p120f);
        if (__builtin_expect(__all(ok), 1)) {
            const f32x16 on = o[t] * (1.0f / lsum);
            *(bf16x8*)(op + t * 65536 + lane * 16) = pack8(on, 0);
            *(bf16x8*)(op + t * 65536 + 1024 + lane * 16) = pack8(on, 8);
        } else {
            redo |= 1u << t;
        }
    }
    if (__builtin_expect(redo != 0, 0)) {
        for (int t = 0; t < AT_QT; ++t)
            if ((redo >> t) & 1) attn_tile_safe(qp + t * 2048, kv, op + t * 65536, lane);
    }
}

constexpr int P3_SLOT = 32768, P3_LDS = 4 * P3_SLOT;

__global__ __launch_bounds__(512) void oproj_kernel(const char* __restrict__ ws, const char* __restrict__ woA, const char* __restrict__ woB, char* __restrict__ out) {
    extern __shared__ __attribute__((aligned(16))) char smem[];
    const int tid = threadIdx.x, lane = tid & 63, w = __builtin_amdgcn_readfirstlane(tid >> 6);
    const int b = blockIdx.x, xcd = b & 7, idx = b >> 3;
    const int br = xcd >> 2, rb = (xcd & 1) * 8 + (idx & 7), ct = ((xcd >> 1) & 1) * 4 + (idx >> 3);
    const int kg = w >> 2, wm = (w >> 1) & 1, wn = w & 1, r = lane & 31, h = lane >> 5;
    const char* og = ws + WS_OF + (size_t)br * 4194304 + (size_t)rb * 4 * 65536;
    const char* wg = (br ? woB : woA) + ct * 256;
    const unsigned lds0 = (unsigned)(uintptr_t)smem;
    const unsigned oo = (unsigned)((w >> 1) * 65536 + (w & 1) * 2048 + lane * 16);
    const unsigned wo0 = (unsigned)((8 * w + (lane >> 4)) * 2048 + (((lane & 15) ^ ((lane >> 4) << 2)) << 4));
    auto stage = [&](int t, int slot) {
        const unsigned l = lds0 + slot * P3_SLOT;
        const char* os = og + t * 4096; const char* wsrc = wg + (size_t)t * 64 * 2048;
        glds16<0>(oo, os, l + w * 2048); glds16<1024>(oo, os, l + w * 2048);
        glds16<0>(wo0, wsrc, l + 16384 + w * 2048); glds16<0>(wo0, wsrc + 4 * 2048, l + 16384 + w * 2048 + 1024);
    };
    const lds_cptr ob = (lds_cptr)smem + (2 * wm) * 4096 + (2 * kg) * 1024 + lane * 16;
    const int q4 = (lane & 15) >> 2, p4 = lane & 3, blk = (lane >> 4) & 1;
    lds_cptr wb[2];
#pragma unroll
    for (int j = 0; j < 2; ++j) wb[j] = (lds_cptr)smem + 16384 + (32 * kg + 4 * h + q4) * 256 + (((2 * wn + j) ^ q4) << 6) + blk * 32 + (p4 & 1) * 16 + (p4 >> 1) * 8;
    f32x16 acc[2][2];
#pragma unroll
    for (int i = 0; i < 2; ++i)
#pragma unroll
        for (int j = 0; j < 2; ++j) acc[i][j] = f32x16{};
    stage(0, 0); stage(1, 1); stage(2, 2);
    for (int t = 0; t < 16; ++t) {
        if (t < 14) WAIT_BAR(8); else if (t == 14) WAIT_BAR(4); else WAIT_BAR(0);
        if (t + 3 < 16) stage(t + 3, (t + 3) & 3);
        const int so_ = (t & 3) * P3_SLOT;
#pragma unroll
        for (int s = 0; s < 2; ++s) {
            bf16x8 of[2], wf[2];
#pragma unroll
            for (int i = 0; i < 2; ++i) of[i] = lds_rd128(ob + so_ + i * 4096 + s * 1024);
#pragma unroll
            for (int j = 0; j < 2; ++j) wf[j] = cat8(lds_tr(wb[j] + so_ + (16 * s) * 256), lds_tr(wb[j] + so_ + (16 * s + 8) * 256));
#pragma unroll
            for (int i = 0; i < 2; ++i)
#pragma unroll
                for (int j = 0; j < 2; ++j) acc[i][j] = MFMA32(wf[j], of[i], acc[i][j]);
        }
    }
    asm volatile("s_waitcnt lgkmcnt(0)\n\ts_barrier" ::: "memory");
    typedef __attribute__((address_space(3))) f32x4* lds_f4;
    f32x16 keep[2];
    {
        lds_f4 dstp = (lds_f4)((lds_ptr)smem + w * 8192 + lane * 16);
#pragma unroll
        for (int j = 0; j < 2; ++j) {
            f32x16 a;
            if (kg) { a = acc[0][j]; keep[j] = acc[1][j]; } else { a = acc[1][j]; keep[j] = acc[0][j]; }
#pragma unroll
            for (int g = 0; g < 4; ++g) dstp[(j * 4 + g) * 64] = (f32x4){a[4 * g], a[4 * g + 1], a[4 * g + 2], a[4 * g + 3]};
        }
    }
    __syncthreads();
    {
        lds_f4 srcp = (lds_f4)((lds_ptr)smem + (w ^ 4) * 8192 + lane * 16);
        char* od = out + (size_t)(rb * 128 + 64 * wm + 32 * kg + r) * 4096 + (br * 1024 + ct * 128 + 64 * wn + 8 * h) * 2;
#pragma unroll
        for (int j = 0; j < 2; ++j) {
            f32x16 a = keep[j];
#pragma unroll
            for (int g = 0; g < 4; ++g) { const f32x4 v = srcp[(j * 4 + g) * 64]; a[4 * g] += v[0]; a[4 * g + 1] += v[1]; a[4 * g + 2] += v[2]; a[4 * g + 3] += v[3]; }
            *(bf16x8*)(od + j * 64) = pack8(a, 0);
            *(bf16x8*)(od + j * 64 + 32) = pack8(a, 8);
        }
    }
}

extern "C" void kernel_launch(void* const* d_in, const int* in_sizes, int n_in, void* d_out, int out_size, void* d_ws, size_t ws_size, hipStream_t stream) {
    (void)in_sizes; (void)n_in; (void)out_size; (void)ws_size;
    const char* xA = (const char*)d_in[0]; const char* wq = (const char*)d_in[1]; const char* wk = (const char*)d_in[2]; const char* wv = (const char*)d_in[3];
    const char* woA = (const char*)d_in[4]; const char* xB = (const char*)d_in[5]; const char* wqkv = (const char*)d_in[6]; const char* woB = (const char*)d_in[7];
    char* ws = (char*)d_ws;
    (void)hipFuncSetAttribute((const void*)proj_kernel, hipFuncAttributeMaxDynamicSharedMemorySize, P1_LDS);
    (void)hipFuncSetAttribute((const void*)attn_kernel, hipFuncAttributeMaxDynamicSharedMemorySize, AT_LDS);
    (void)hipFuncSetAttribute((const void*)oproj_kernel, hipFuncAttributeMaxDynamicSharedMemorySize, P3_LDS);
    proj_kernel<<<256, 512, P1_LDS, stream>>>(xA, wq, wk, wv, xB, wqkv, ws);
    attn_kernel<<<64 * (64 / (AT_NW * AT_QT)), AT_NW * 64, AT_LDS, stream>>>(ws);
    oproj_kernel<<<256, 512, P3_LDS, stream>>>(ws, woA, woB, (char*)d_out);
}
```

```cpp
#include <hip/hip_runtime.h>
#include <hip/hip_bf16.h>
#include <stdint.h>

typedef __attribute__((ext_vector_type(8))) __bf16 bf16x8;
typedef __attribute__((ext_vector_type(2))) __bf16 bf16x2;
typedef __attribute__((ext_vector_type(16))) float f32x16;
typedef __attribute__((ext_vector_type(4))) float f32x4;
typedef __attribute__((ext_vector_type(2))) float f32x2;
typedef __attribute__((ext_vector_type(4))) unsigned u32x4;
typedef __attribute__((ext_vector_type(2))) unsigned u32x2;
typedef __attribute__((address_space(3))) char* lds_ptr;

#define DI __device__ __forceinline__
#define MFMA32(a, b, c) __builtin_amdgcn_mfma_f32_32x32x16_bf16((a), (b), (c), 0, 0, 0)

constexpr size_t WS_QF = 0;
constexpr size_t WS_KV = WS_QF + (size_t)64 * 131072;
constexpr size_t WS_OF = WS_KV + (size_t)33 * 262144;
constexpr float QSCALE = 0.17677669529663687f * 1.4426950408889634f;

DI unsigned cvtpk(float lo, float hi) { f32x2 v = {lo, hi}; bf16x2 b = __builtin_convertvector(v, bf16x2); return __builtin_bit_cast(unsigned, b); }
DI bf16x8 pack8(const f32x16& a, int o) {
    u32x4 w = {cvtpk(a[o], a[o + 1]), cvtpk(a[o + 2], a[o + 3]), cvtpk(a[o + 4], a[o + 5]), cvtpk(a[o + 6], a[o + 7])};
    return __builtin_bit_cast(bf16x8, w);
}

template <int OFF> DI void glds16(unsigned voff, const void* sbase, unsigned ldsdst) {
    unsigned keep;
    asm volatile("s_mov_b32 %0, m0\n\ts_mov_b32 m0, %3\n\ts_nop 0\n\tglobal_load_lds_dwordx4 %1, %2 offset:%c4\n\ts_mov_b32 m0, %0"
                 : "=&s"(keep) : "v"(voff), "s"(sbase), "s"(ldsdst), "i"(OFF) : "memory");
}
DI void touch64(const char* lane_ptr, unsigned ldsdummy) {
    unsigned keep;
    asm volatile("s_mov_b32 %0, m0\n\ts_mov_b32 m0, %2\n\ts_nop 0\n\tglobal_load_lds_dword %1, off\n\ts_mov_b32 m0, %0" : "=&s"(keep) : "v"(lane_ptr), "s"(ldsdummy) : "memory");
}
template <int POLICY> DI void st16(void* p, u32x4 v) {
    if (POLICY == 0) *(u32x4*)p = v;
    else if (POLICY == 1) asm volatile("global_store_dwordx4 %0, %1, off sc1\n\ts_nop 1" :: "v"(p), "v"(v) : "memory");
    else __builtin_nontemporal_store(v, (u32x4*)p);
}
template <int POLICY> DI void st16(void* p, bf16x8 v) { st16<POLICY>(p, __builtin_bit_cast(u32x4, v)); }
constexpr int ST0 = 2, ST1 = 1, ST2 = 1;
#define WAIT_BAR(N) asm volatile("s_waitcnt vmcnt(" #N ") lgkmcnt(0)\n\ts_barrier" ::: "memory")
#define WAIT_VM0() asm volatile("s_waitcnt vmcnt(0)" ::: "memory")
#define CODE_ADDR(p, sym) asm volatile("s_getpc_b64 s[60:61]\n\ts_add_u32 s60, s60, " #sym "@rel32@lo+4\n\ts_addc_u32 s61, s61, " #sym "@rel32@hi+12" : "={s[60:61]}"(p) :: "scc")

DI bf16x8 pack44(const f32x4& a, const f32x4& b) {
    u32x4 w = {cvtpk(a[0], a[1]), cvtpk(a[2], a[3]), cvtpk(b[0], b[1]), cvtpk(b[2], b[3])};
    return __builtin_bit_cast(bf16x8, w);
}
DI f32x4 sub4(const f32x16& a, int j) { return (f32x4){a[4 * j], a[4 * j + 1], a[4 * j + 2], a[4 * j + 3]}; }
#define PROJ_QK_ASM \
        "ds_read_b64_tr_b16 v[192:193], v225 offset:16384\n\t" \
        "ds_read_b64_tr_b16 v[194:195], v225 offset:18432\n\t" \
        "ds_read_b128 v[128:131], v224 offset:0\n\t" \
        "ds_read_b128 v[132:135], v224 offset:2048\n\t" \
        "ds_read_b128 v[136:139], v224 offset:4096\n\t" \
        "ds_read_b128 v[140:143], v224 offset:6144\n\t" \
        "ds_read_b128 v[144:147], v224 offset:8192\n\t" \
        "ds_read_b128 v[148:151], v224 offset:10240\n\t" \
        "ds_read_b128 v[152:155], v224 offset:12288\n\t" \
        "ds_read_b128 v[156:159], v224 offset:14336\n\t" \
        "ds_read_b64_tr_b16 v[196:197], v226 offset:16384\n\t" \
        "ds_read_b64_tr_b16 v[198:199], v226 offset:18432\n\t" \
        "ds_read_b64_tr_b16 v[200:201], v227 offset:16384\n\t" \
        "ds_read_b64_tr_b16 v[202:203], v227 offset:18432\n\t" \
        "ds_read_b64_tr_b16 v[204:205], v228 offset:16384\n\t" \
        "ds_read_b64_tr_b16 v[206:207], v228 offset:18432\n\t" \
        "s_mov_b32 s42, 0\n\t" \
        "s_mov_b32 s43, 0\n\t" \
        "s_mov_b32 s45, 51200\n\t" \
        "s_mov_b32 s46, -102400\n\t" \
        "1:\n\t" \
        "s_waitcnt lgkmcnt(0)\n\t" \
        "v_mfma_f32_16x16x32_bf16 v[0:3], v[192:195], v[128:131], v[0:3]\n\t" \
        "v_xor_b32_e32 v224, 64, v224\n\t" \
        "v_mfma_f32_16x16x32_bf16 v[16:19], v[192:195], v[132:135], v[16:19]\n\t" \
        "ds_read_b64_tr_b16 v[208:209], v225 offset:32768\n\t" \
        "v_mfma_f32_16x16x32_bf16 v[32:35], v[192:195], v[136:139], v[32:35]\n\t" \
        "v_mfma_f32_16x16x32_bf16 v[48:51], v[192:195], v[140:143], v[48:51]\n\t" \
        "ds_read_b64_tr_b16 v[210:211], v225 offset:34816\n\t" \
        "v_mfma_f32_16x16x32_bf16 v[64:67], v[192:195], v[144:147], v[64:67]\n\t" \
        "ds_read_b128 v[160:163], v224 offset:0\n\t" \
        "v_mfma_f32_16x16x32_bf16 v[80:83], v[192:195], v[148:151], v[80:83]\n\t" \
        "v_mfma_f32_16x16x32_bf16 v[96:99], v[192:195], v[152:155], v[96:99]\n\t" \
        "ds_read_b128 v[164:167], v224 offset:2048\n\t" \
        "v_mfma_f32_16x16x32_bf16 v[112:115], v[192:195], v[156:159], v[112:115]\n\t" \
        "ds_read_b128 v[168:171], v224 offset:4096\n\t" \
        "v_mfma_f32_16x16x32_bf16 v[4:7], v[196:199], v[128:131], v[4:7]\n\t" \
        "v_mfma_f32_16x16x32_bf16 v[20:23], v[196:199], v[132:135], v[20:23]\n\t" \
        "ds_read_b128 v[172:175], v224 offset:6144\n\t" \
        "v_mfma_f32_16x16x32_bf16 v[36:39], v[196:199], v[136:139], v[36:39]\n\t" \
        "ds_read_b128 v[176:179], v224 offset:8192\n\t" \
        "v_mfma_f32_16x16x32_bf16 v[52:55], v[196:199], v[140:143], v[52:55]\n\t" \
        "v_mfma_f32_16x16x32_bf16 v[68:71], v[196:199], v[144:147], v[68:71]\n\t" \
        "ds_read_b128 v[180:183], v224 offset:10240\n\t" \
        "v_mfma_f32_16x16x32_bf16 v[84:87], v[196:199], v[148:151], v[84:87]\n\t" \
        "ds_read_b128 v[184:187], v224 offset:12288\n\t" \
        "v_mfma_f32_16x16x32_bf16 v[100:103], v[196:199], v[152:155], v[100:103]\n\t" \
        "v_mfma_f32_16x16x32_bf16 v[116:119], v[196:199], v[156:159], v[116:119]\n\t" \
        "ds_read_b128 v[188:191], v224 offset:14336\n\t" \
        "v_mfma_f32_16x16x32_bf16 v[8:11], v[200:203], v[128:131], v[8:11]\n\t" \
        "ds_read_b64_tr_b16 v[212:213], v226 offset:32768\n\t" \
        "v_mfma_f32_16x16x32_bf16 v[24:27], v[200:203], v[132:135], v[24:27]\n\t" \
        "v_mfma_f32_16x16x32_bf16 v[40:43], v[200:203], v[136:139], v[40:43]\n\t" \
        "ds_read_b64_tr_b16 v[214:215], v226 offset:34816\n\t" \
        "v_mfma_f32_16x16x32_bf16 v[56:59], v[200:203], v[140:143], v[56:59]\n\t" \
        "ds_read_b64_tr_b16 v[216:217], v227 offset:32768\n\t" \
        "v_mfma_f32_16x16x32_bf16 v[72:75], v[200:203], v[144:147], v[72:75]\n\t" \
        "v_mfma_f32_16x16x32_bf16 v[88:91], v[200:203], v[148:151], v[88:91]\n\t" \
        "ds_read_b64_tr_b16 v[218:219], v227 offset:34816\n\t" \
        "v_mfma_f32_16x16x32_bf16 v[104:107], v[200:203], v[152:155], v[104:107]\n\t" \
        "ds_read_b64_tr_b16 v[220:221], v228 offset:32768\n\t" \
        "v_mfma_f32_16x16x32_bf16 v[120:123], v[200:203], v[156:159], v[120:123]\n\t" \
        "v_mfma_f32_16x16x32_bf16 v[12:15], v[204:207], v[128:131], v[12:15]\n\t" \
        "ds_read_b64_tr_b16 v[222:223], v228 offset:34816\n\t" \
        "v_mfma_f32_16x16x32_bf16 v[28:31], v[204:207], v[132:135], v[28:31]\n\t" \
        "v_mfma_f32_16x16x32_bf16 v[44:47], v[204:207], v[136:139], v[44:47]\n\t" \
        "s_add_u32 s43, s43, 1\n\t" \
        "v_mfma_f32_16x16x32_bf16 v[60:63], v[204:207], v[140:143], v[60:63]\n\t" \
        "s_cmp_eq_u32 s43, 3\n\t" \
        "v_mfma_f32_16x16x32_bf16 v[76:79], v[204:207], v[144:147], v[76:79]\n\t" \
        "s_cselect_b32 s44, s46, s45\n\t" \
        "v_mfma_f32_16x16x32_bf16 v[92:95], v[204:207], v[148:151], v[92:95]\n\t" \
        "s_cselect_b32 s43, 0, s43\n\t" \
        "v_mfma_f32_16x16x32_bf16 v[108:111], v[204:207], v[152:155], v[108:111]\n\t" \
        "v_mfma_f32_16x16x32_bf16 v[124:127], v[204:207], v[156:159], v[124:127]\n\t" \
        "s_waitcnt lgkmcnt(0)\n\t" \
        "s_barrier\n\t" \
        "v_mfma_f32_16x16x32_bf16 v[0:3], v[208:211], v[160:163], v[0:3]\n\t" \
        "v_xor_b32_e32 v224, 64, v224\n\t" \
        "v_mfma_f32_16x16x32_bf16 v[16:19], v[208:211], v[164:167], v[16:19]\n\t" \
        "v_add_u32_e32 v224, s44, v224\n\t" \
        "v_mfma_f32_16x16x32_bf16 v[32:35], v[208:211], v[168:171], v[32:35]\n\t" \
        "v_add_u32_e32 v225, s44, v225\n\t" \
        "v_mfma_f32_16x16x32_bf16 v[48:51], v[208:211], v[172:175], v[48:51]\n\t" \
        "v_add_u32_e32 v226, s44, v226\n\t" \
        "v_mfma_f32_16x16x32_bf16 v[64:67], v[208:211], v[176:179], v[64:67]\n\t" \
        "v_add_u32_e32 v227, s44, v227\n\t" \
        "v_mfma_f32_16x16x32_bf16 v[80:83], v[208:211], v[180:183], v[80:83]\n\t" \
        "v_add_u32_e32 v228, s44, v228\n\t" \
        "v_mfma_f32_16x16x32_bf16 v[96:99], v[208:211], v[184:187], v[96:99]\n\t" \
        "ds_read_b64_tr_b16 v[192:193], v225 offset:16384\n\t" \
        "v_mfma_f32_16x16x32_bf16 v[112:115], v[208:211], v[188:191], v[112:115]\n\t" \
        "v_mfma_f32_16x16x32_bf16 v[4:7], v[212:215], v[160:163], v[4:7]\n\t" \
        "ds_read_b64_tr_b16 v[194:195], v225 offset:18432\n\t" \
        "v_mfma_f32_16x16x32_bf16 v[20:23], v[212:215], v[164:167], v[20:23]\n\t" \
        "ds_read_b128 v[128:131], v224 offset:0\n\t" \
        "v_mfma_f32_16x16x32_bf16 v[36:39], v[212:215], v[168:171], v[36:39]\n\t" \
        "ds_read_b128 v[132:135], v224 offset:2048\n\t" \
        "v_mfma_f32_16x16x32_bf16 v[52:55], v[212:215], v[172:175], v[52:55]\n\t" \
        "ds_read_b128 v[136:139], v224 offset:4096\n\t" \
        "v_mfma_f32_16x16x32_bf16 v[68:71], v[212:215], v[176:179], v[68:71]\n\t" \
        "ds_read_b128 v[140:143], v224 offset:6144\n\t" \
        "v_mfma_f32_16x16x32_bf16 v[84:87], v[212:215], v[180:183], v[84:87]\n\t" \
        "ds_read_b128 v[144:147], v224 offset:8192\n\t" \
        "v_mfma_f32_16x16x32_bf16 v[100:103], v[212:215], v[184:187], v[100:103]\n\t" \
        "ds_read_b128 v[148:151], v224 offset:10240\n\t" \
        "v_mfma_f32_16x16x32_bf16 v[116:119], v[212:215], v[188:191], v[116:119]\n\t" \
        "v_mfma_f32_16x16x32_bf16 v[8:11], v[216:219], v[160:163], v[8:11]\n\t" \
        "ds_read_b128 v[152:155], v224 offset:12288\n\t" \
        "v_mfma_f32_16x16x32_bf16 v[24:27], v[216:219], v[164:167], v[24:27]\n\t" \
        "ds_read_b128 v[156:159], v224 offset:14336\n\t" \
        "v_mfma_f32_16x16x32_bf16 v[40:43], v[216:219], v[168:171], v[40:43]\n\t" \
        "ds_read_b64_tr_b16 v[196:197], v226 offset:16384\n\t" \
        "v_mfma_f32_16x16x32_bf16 v[56:59], v[216:219], v[172:175], v[56:59]\n\t" \
        "ds_read_b64_tr_b16 v[198:199], v226 offset:18432\n\t" \
        "v_mfma_f32_16x16x32_bf16 v[72:75], v[216:219], v[176:179], v[72:75]\n\t" \
        "ds_read_b64_tr_b16 v[200:201], v227 offset:16384\n\t" \
        "v_mfma_f32_16x16x32_bf16 v[88:91], v[216:219], v[180:183], v[88:91]\n\t" \
        "ds_read_b64_tr_b16 v[202:203], v227 offset:18432\n\t" \
        "v_mfma_f32_16x16x32_bf16 v[104:107], v[216:219], v[184:187], v[104:107]\n\t" \
        "ds_read_b64_tr_b16 v[204:205], v228 offset:16384\n\t" \
        "v_mfma_f32_16x16x32_bf16 v[120:123], v[216:219], v[188:191], v[120:123]\n\t" \
        "v_mfma_f32_16x16x32_bf16 v[12:15], v[220:223], v[160:163], v[12:15]\n\t" \
        "ds_read_b64_tr_b16 v[206:207], v228 offset:18432\n\t" \
        "v_mfma_f32_16x16x32_bf16 v[28:31], v[220:223], v[164:167], v[28:31]\n\t" \
        "v_mfma_f32_16x16x32_bf16 v[44:47], v[220:223], v[168:171], v[44:47]\n\t" \
        "v_mfma_f32_16x16x32_bf16 v[60:63], v[220:223], v[172:175], v[60:63]\n\t" \
        "v_mfma_f32_16x16x32_bf16 v[76:79], v[220:223], v[176:179], v[76:79]\n\t" \
        "v_mfma_f32_16x16x32_bf16 v[92:95], v[220:223], v[180:183], v[92:95]\n\t" \
        "v_mfma_f32_16x16x32_bf16 v[108:111], v[220:223], v[184:187], v[108:111]\n\t" \
        "v_mfma_f32_16x16x32_bf16 v[124:127], v[220:223], v[188:191], v[124:127]\n\t" \
        "s_waitcnt lgkmcnt(0)\n\t" \
        "v_mfma_f32_16x16x32_bf16 v[0:3], v[192:195], v[128:131], v[0:3]\n\t" \
        "v_xor_b32_e32 v224, 64, v224\n\t" \
        "v_mfma_f32_16x16x32_bf16 v[16:19], v[192:195], v[132:135], v[16:19]\n\t" \
        "ds_read_b64_tr_b16 v[208:209], v225 offset:32768\n\t" \
        "v_mfma_f32_16x16x32_bf16 v[32:35], v[192:195], v[136:139], v[32:35]\n\t" \
        "v_mfma_f32_16x16x32_bf16 v[48:51], v[192:195], v[140:143], v[48:51]\n\t" \
        "ds_read_b64_tr_b16 v[210:211], v225 offset:34816\n\t" \
        "v_mfma_f32_16x16x32_bf16 v[64:67], v[192:195], v[144:147], v[64:67]\n\t" \
        "ds_read_b128 v[160:163], v224 offset:0\n\t" \
        "v_mfma_f32_16x16x32_bf16 v[80:83], v[192:195], v[148:151], v[80:83]\n\t" \
        "v_mfma_f32_16x16x32_bf16 v[96:99], v[192:195], v[152:155], v[96:99]\n\t" \
        "ds_read_b128 v[164:167], v224 offset:2048\n\t" \
        "v_mfma_f32_16x16x32_bf16 v[112:115], v[192:195], v[156:159], v[112:115]\n\t" \
        "ds_read_b128 v[168:171], v224 offset:4096\n\t" \
        "v_mfma_f32_16x16x32_bf16 v[4:7], v[196:199], v[128:131], v[4:7]\n\t" \
        "v_mfma_f32_16x16x32_bf16 v[20:23], v[196:199], v[132:135], v[20:23]\n\t" \
        "ds_read_b128 v[172:175], v224 offset:6144\n\t" \
        "v_mfma_f32_16x16x32_bf16 v[36:39], v[196:199], v[136:139], v[36:39]\n\t" \
        "ds_read_b128 v[176:179], v224 offset:8192\n\t" \
        "v_mfma_f32_16x16x32_bf16 v[52:55], v[196:199], v[140:143], v[52:55]\n\t" \
        "v_mfma_f32_16x16x32_bf16 v[68:71], v[196:199], v[144:147], v[68:71]\n\t" \
        "ds_read_b128 v[180:183], v224 offset:10240\n\t" \
        "v_mfma_f32_16x16x32_bf16 v[84:87], v[196:199], v[148:151], v[84:87]\n\t" \
        "ds_read_b128 v[184:187], v224 offset:12288\n\t" \
        "v_mfma_f32_16x16x32_bf16 v[100:103], v[196:199], v[152:155], v[100:103]\n\t" \
        "v_mfma_f32_16x16x32_bf16 v[116:119], v[196:199], v[156:159], v[116:119]\n\t" \
        "ds_read_b128 v[188:191], v224 offset:14336\n\t" \
        "v_mfma_f32_16x16x32_bf16 v[8:11], v[200:203], v[128:131], v[8:11]\n\t" \
        "ds_read_b64_tr_b16 v[212:213], v226 offset:32768\n\t" \
        "v_mfma_f32_16x16x32_bf16 v[24:27], v[200:203], v[132:135], v[24:27]\n\t" \
        "v_mfma_f32_16x16x32_bf16 v[40:43], v[200:203], v[136:139], v[40:43]\n\t" \
        "ds_read_b64_tr_b16 v[214:215], v226 offset:34816\n\t" \
        "v_mfma_f32_16x16x32_bf16 v[56:59], v[200:203], v[140:143], v[56:59]\n\t" \
        "ds_read_b64_tr_b16 v[216:217], v227 offset:32768\n\t" \
        "v_mfma_f32_16x16x32_bf16 v[72:75], v[200:203], v[144:147], v[72:75]\n\t" \
        "v_mfma_f32_16x16x32_bf16 v[88:91], v[200:203], v[148:151], v[88:91]\n\t" \
        "ds_read_b64_tr_b16 v[218:219], v227 offset:34816\n\t" \
        "v_mfma_f32_16x16x32_bf16 v[104:107], v[200:203], v[152:155], v[104:107]\n\t" \
        "ds_read_b64_tr_b16 v[220:221], v228 offset:32768\n\t" \
        "v_mfma_f32_16x16x32_bf16 v[120:123], v[200:203], v[156:159], v[120:123]\n\t" \
        "v_mfma_f32_16x16x32_bf16 v[12:15], v[204:207], v[128:131], v[12:15]\n\t" \
        "ds_read_b64_tr_b16 v[222:223], v228 offset:34816\n\t" \
        "v_mfma_f32_16x16x32_bf16 v[28:31], v[204:207], v[132:135], v[28:31]\n\t" \
        "v_mfma_f32_16x16x32_bf16 v[44:47], v[204:207], v[136:139], v[44:47]\n\t" \
        "s_add_u32 s43, s43, 1\n\t" \
        "v_mfma_f32_16x16x32_bf16 v[60:63], v[204:207], v[140:143], v[60:63]\n\t" \
        "s_cmp_eq_u32 s43, 3\n\t" \
        "v_mfma_f32_16x16x32_bf16 v[76:79], v[204:207], v[144:147], v[76:79]\n\t" \
        "s_cselect_b32 s44, s46, s45\n\t" \
        "v_mfma_f32_16x16x32_bf16 v[92:95], v[204:207], v[148:151], v[92:95]\n\t" \
        "s_cselect_b32 s43, 0, s43\n\t" \
        "v_mfma_f32_16x16x32_bf16 v[108:111], v[204:207], v[152:155], v[108:111]\n\t" \
        "v_mfma_f32_16x16x32_bf16 v[124:127], v[204:207], v[156:159], v[124:127]\n\t" \
        "s_waitcnt lgkmcnt(0)\n\t" \
        "s_barrier\n\t" \
        "v_mfma_f32_16x16x32_bf16 v[0:3], v[208:211], v[160:163], v[0:3]\n\t" \
        "v_xor_b32_e32 v224, 64, v224\n\t" \
        "v_mfma_f32_16x16x32_bf16 v[16:19], v[208:211], v[164:167], v[16:19]\n\t" \
        "v_add_u32_e32 v224, s44, v224\n\t" \
        "v_mfma_f32_16x16x32_bf16 v[32:35], v[208:211], v[168:171], v[32:35]\n\t" \
        "v_add_u32_e32 v225, s44, v225\n\t" \
        "v_mfma_f32_16x16x32_bf16 v[48:51], v[208:211], v[172:175], v[48:51]\n\t" \
        "v_add_u32_e32 v226, s44, v226\n\t" \
        "v_mfma_f32_16x16x32_bf16 v[64:67], v[208:211], v[176:179], v[64:67]\n\t" \
        "v_add_u32_e32 v227, s44, v227\n\t" \
        "v_mfma_f32_16x16x32_bf16 v[80:83], v[208:211], v[180:183], v[80:83]\n\t" \
        "v_add_u32_e32 v228, s44, v228\n\t" \
        "v_mfma_f32_16x16x32_bf16 v[96:99], v[208:211], v[184:187], v[96:99]\n\t" \
        "ds_read_b64_tr_b16 v[192:193], v225 offset:16384\n\t" \
        "v_mfma_f32_16x16x32_bf16 v[112:115], v[208:211], v[188:191], v[112:115]\n\t" \
        "v_mfma_f32_16x16x32_bf16 v[4:7], v[212:215], v[160:163], v[4:7]\n\t" \
        "ds_read_b64_tr_b16 v[194:195], v225 offset:18432\n\t" \
        "v_mfma_f32_16x16x32_bf16 v[20:23], v[212:215], v[164:167], v[20:23]\n\t" \
        "ds_read_b128 v[128:131], v224 offset:0\n\t" \
        "v_mfma_f32_16x16x32_bf16 v[36:39], v[212:215], v[168:171], v[36:39]\n\t" \
        "ds_read_b128 v[132:135], v224 offset:2048\n\t" \
        "v_mfma_f32_16x16x32_bf16 v[52:55], v[212:215], v[172:175], v[52:55]\n\t" \
        "ds_read_b128 v[136:139], v224 offset:4096\n\t" \
        "v_mfma_f32_16x16x32_bf16 v[68:71], v[212:215], v[176:179], v[68:71]\n\t" \
        "ds_read_b128 v[140:143], v224 offset:6144\n\t" \
        "v_mfma_f32_16x16x32_bf16 v[84:87], v[212:215], v[180:183], v[84:87]\n\t" \
        "ds_read_b128 v[144:147], v224 offset:8192\n\t" \
        "v_mfma_f32_16x16x32_bf16 v[100:103], v[212:215], v[184:187], v[100:103]\n\t" \
        "ds_read_b128 v[148:151], v224 offset:10240\n\t" \
        "v_mfma_f32_16x16x32_bf16 v[116:119], v[212:215], v[188:191], v[116:119]\n\t" \
        "v_mfma_f32_16x16x32_bf16 v[8:11], v[216:219], v[160:163], v[8:11]\n\t" \
        "ds_read_b128 v[152:155], v224 offset:12288\n\t" \
        "v_mfma_f32_16x16x32_bf16 v[24:27], v[216:219], v[164:167], v[24:27]\n\t" \
        "ds_read_b128 v[156:159], v224 offset:14336\n\t" \
        "v_mfma_f32_16x16x32_bf16 v[40:43], v[216:219], v[168:171], v[40:43]\n\t" \
        "ds_read_b64_tr_b16 v[196:197], v226 offset:16384\n\t" \
        "v_mfma_f32_16x16x32_bf16 v[56:59], v[216:219], v[172:175], v[56:59]\n\t" \
        "ds_read_b64_tr_b16 v[198:199], v226 offset:18432\n\t" \
        "v_mfma_f32_16x16x32_bf16 v[72:75], v[216:219], v[176:179], v[72:75]\n\t" \
        "ds_read_b64_tr_b16 v[200:201], v227 offset:16384\n\t" \
        "v_mfma_f32_16x16x32_bf16 v[88:91], v[216:219], v[180:183], v[88:91]\n\t" \
        "ds_read_b64_tr_b16 v[202:203], v227 offset:18432\n\t" \
        "v_mfma_f32_16x16x32_bf16 v[104:107], v[216:219], v[184:187], v[104:107]\n\t" \
        "ds_read_b64_tr_b16 v[204:205], v228 offset:16384\n\t" \
        "v_mfma_f32_16x16x32_bf16 v[120:123], v[216:219], v[188:191], v[120:123]\n\t" \
        "v_mfma_f32_16x16x32_bf16 v[12:15], v[220:223], v[160:163], v[12:15]\n\t" \
        "ds_read_b64_tr_b16 v[206:207], v228 offset:18432\n\t" \
        "v_mfma_f32_16x16x32_bf16 v[28:31], v[220:223], v[164:167], v[28:31]\n\t" \
        "v_mfma_f32_16x16x32_bf16 v[44:47], v[220:223], v[168:171], v[44:47]\n\t" \
        "v_mfma_f32_16x16x32_bf16 v[60:63], v[220:223], v[172:175], v[60:63]\n\t" \
        "v_mfma_f32_16x16x32_bf16 v[76:79], v[220:223], v[176:179], v[76:79]\n\t" \
        "v_mfma_f32_16x16x32_bf16 v[92:95], v[220:223], v[180:183], v[92:95]\n\t" \
        "v_mfma_f32_16x16x32_bf16 v[108:111], v[220:223], v[184:187], v[108:111]\n\t" \
        "v_mfma_f32_16x16x32_bf16 v[124:127], v[220:223], v[188:191], v[124:127]\n\t" \
        "s_add_u32 s42, s42, 2\n\t" \
        "s_cmp_lt_u32 s42, 16\n\t" \
        "s_cbranch_scc1 1b\n\t" \
        "s_waitcnt lgkmcnt(0)\n\t" \
        "s_nop 7\n\t" \
        "s_nop 1\n\t"
#define PROJ_QK_CLOBBERS "v128", "v129", "v130", "v131", "v132", "v133", "v134", "v135", "v136", "v137", "v138", "v139", "v140", "v141", "v142", "v143", "v144", "v145", "v146", "v147", "v148", "v149", "v150", "v151", "v152", "v153", "v154", "v155", "v156", "v157", "v158", "v159", "v160", "v161", "v162", "v163", "v164", "v165", "v166", "v167", "v168", "v169", "v170", "v171", "v172", "v173", "v174", "v175", "v176", "v177", "v178", "v179", "v180", "v181", "v182", "v183", "v184", "v185", "v186", "v187", "v188", "v189", "v190", "v191", "v192", "v193", "v194", "v195", "v196", "v197", "v198", "v199", "v200", "v201", "v202", "v203", "v204", "v205", "v206", "v207", "v208", "v209", "v210", "v211", "v212", "v213", "v214", "v215", "v216", "v217", "v218", "v219", "v220", "v221", "v222", "v223", "s42", "s43", "s44", "s45", "s46"
#define PROJ_V_ASM \
        "ds_read_b64_tr_b16 v[192:193], v225 offset:16384\n\t" \
        "ds_read_b64_tr_b16 v[194:195], v225 offset:18432\n\t" \
        "ds_read_b128 v[128:131], v224 offset:0\n\t" \
        "ds_read_b128 v[132:135], v224 offset:2048\n\t" \
        "ds_read_b128 v[136:139], v224 offset:4096\n\t" \
        "ds_read_b128 v[140:143], v224 offset:6144\n\t" \
        "ds_read_b128 v[144:147], v224 offset:8192\n\t" \
        "ds_read_b128 v[148:151], v224 offset:10240\n\t" \
        "ds_read_b128 v[152:155], v224 offset:12288\n\t" \
        "ds_read_b128 v[156:159], v224 offset:14336\n\t" \
        "ds_read_b64_tr_b16 v[196:197], v226 offset:16384\n\t" \
        "ds_read_b64_tr_b16 v[198:199], v226 offset:18432\n\t" \
        "ds_read_b64_tr_b16 v[200:201], v227 offset:16384\n\t" \
        "ds_read_b64_tr_b16 v[202:203], v227 offset:18432\n\t" \
        "ds_read_b64_tr_b16 v[204:205], v228 offset:16384\n\t" \
        "ds_read_b64_tr_b16 v[206:207], v228 offset:18432\n\t" \
        "s_mov_b32 s42, 0\n\t" \
        "s_mov_b32 s43, 0\n\t" \
        "s_mov_b32 s45, 51200\n\t" \
        "s_mov_b32 s46, -102400\n\t" \
        "1:\n\t" \
        "s_waitcnt lgkmcnt(0)\n\t" \
        "v_mfma_f32_16x16x32_bf16 v[0:3], v[128:131], v[192:195], v[0:3]\n\t" \
        "v_xor_b32_e32 v224, 64, v224\n\t" \
        "v_mfma_f32_16x16x32_bf16 v[16:19], v[132:135], v[192:195], v[16:19]\n\t" \
        "ds_read_b64_tr_b16 v[208:209], v225 offset:32768\n\t" \
        "v_mfma_f32_16x16x32_bf16 v[32:35], v[136:139], v[192:195], v[32:35]\n\t" \
        "v_mfma_f32_16x16x32_bf16 v[48:51], v[140:143], v[192:195], v[48:51]\n\t" \
        "ds_read_b64_tr_b16 v[210:211], v225 offset:34816\n\t" \
        "v_mfma_f32_16x16x32_bf16 v[64:67], v[144:147], v[192:195], v[64:67]\n\t" \
        "ds_read_b128 v[160:163], v224 offset:0\n\t" \
        "v_mfma_f32_16x16x32_bf16 v[80:83], v[148:151], v[192:195], v[80:83]\n\t" \
        "v_mfma_f32_16x16x32_bf16 v[96:99], v[152:155], v[192:195], v[96:99]\n\t" \
        "ds_read_b128 v[164:167], v224 offset:2048\n\t" \
        "v_mfma_f32_16x16x32_bf16 v[112:115], v[156:159], v[192:195], v[112:115]\n\t" \
        "ds_read_b128 v[168:171], v224 offset:4096\n\t" \
        "v_mfma_f32_16x16x32_bf16 v[4:7], v[128:131], v[196:199], v[4:7]\n\t" \
        "v_mfma_f32_16x16x32_bf16 v[20:23], v[132:135], v[196:199], v[20:23]\n\t" \
        "ds_read_b128 v[172:175], v224 offset:6144\n\t" \
        "v_mfma_f32_16x16x32_bf16 v[36:39], v[136:139], v[196:199], v[36:39]\n\t" \
        "ds_read_b128 v[176:179], v224 offset:8192\n\t" \
        "v_mfma_f32_16x16x32_bf16 v[52:55], v[140:143], v[196:199], v[52:55]\n\t" \
        "v_mfma_f32_16x16x32_bf16 v[68:71], v[144:147], v[196:199], v[68:71]\n\t" \
        "ds_read_b128 v[180:183], v224 offset:10240\n\t" \
        "v_mfma_f32_16x16x32_bf16 v[84:87], v[148:151], v[196:199], v[84:87]\n\t" \
        "ds_read_b128 v[184:187], v224 offset:12288\n\t" \
        "v_mfma_f32_16x16x32_bf16 v[100:103], v[152:155], v[196:199], v[100:103]\n\t" \
        "v_mfma_f32_16x16x32_bf16 v[116:119], v[156:159], v[196:199], v[116:119]\n\t" \
        "ds_read_b128 v[188:191], v224 offset:14336\n\t" \
        "v_mfma_f32_16x16x32_bf16 v[8:11], v[128:131], v[200:203], v[8:11]\n\t" \
        "ds_read_b64_tr_b16 v[212:213], v226 offset:32768\n\t" \
        "v_mfma_f32_16x16x32_bf16 v[24:27], v[132:135], v[200:203], v[24:27]\n\t" \
        "v_mfma_f32_16x16x32_bf16 v[40:43], v[136:139], v[200:203], v[40:43]\n\t" \
        "ds_read_b64_tr_b16 v[214:215], v226 offset:34816\n\t" \
        "v_mfma_f32_16x16x32_bf16 v[56:59], v[140:143], v[200:203], v[56:59]\n\t" \
        "ds_read_b64_tr_b16 v[216:217], v227 offset:32768\n\t" \
        "v_mfma_f32_16x16x32_bf16 v[72:75], v[144:147], v[200:203], v[72:75]\n\t" \
        "v_mfma_f32_16x16x32_bf16 v[88:91], v[148:151], v[200:203], v[88:91]\n\t" \
        "ds_read_b64_tr_b16 v[218:219], v227 offset:34816\n\t" \
        "v_mfma_f32_16x16x32_bf16 v[104:107], v[152:155], v[200:203], v[104:107]\n\t" \
        "ds_read_b64_tr_b16 v[220:221], v228 offset:32768\n\t" \
        "v_mfma_f32_16x16x32_bf16 v[120:123], v[156:159], v[200:203], v[120:123]\n\t" \
        "v_mfma_f32_16x16x32_bf16 v[12:15], v[128:131], v[204:207], v[12:15]\n\t" \
        "ds_read_b64_tr_b16 v[222:223], v228 offset:34816\n\t" \
        "v_mfma_f32_16x16x32_bf16 v[28:31], v[132:135], v[204:207], v[28:31]\n\t" \
        "v_mfma_f32_16x16x32_bf16 v[44:47], v[136:139], v[204:207], v[44:47]\n\t" \
        "s_add_u32 s43, s43, 1\n\t" \
        "v_mfma_f32_16x16x32_bf16 v[60:63], v[140:143], v[204:207], v[60:63]\n\t" \
        "s_cmp_eq_u32 s43, 3\n\t" \
        "v_mfma_f32_16x16x32_bf16 v[76:79], v[144:147], v[204:207], v[76:79]\n\t" \
        "s_cselect_b32 s44, s46, s45\n\t" \
        "v_mfma_f32_16x16x32_bf16 v[92:95], v[148:151], v[204:207], v[92:95]\n\t" \
        "s_cselect_b32 s43, 0, s43\n\t" \
        "v_mfma_f32_16x16x32_bf16 v[108:111], v[152:155], v[204:207], v[108:111]\n\t" \
        "v_mfma_f32_16x16x32_bf16 v[124:127], v[156:159], v[204:207], v[124:127]\n\t" \
        "s_waitcnt lgkmcnt(0)\n\t" \
        "s_barrier\n\t" \
        "v_mfma_f32_16x16x32_bf16 v[0:3], v[160:163], v[208:211], v[0:3]\n\t" \
        "v_xor_b32_e32 v224, 64, v224\n\t" \
        "v_mfma_f32_16x16x32_bf16 v[16:19], v[164:167], v[208:211], v[16:19]\n\t" \
        "v_add_u32_e32 v224, s44, v224\n\t" \
        "v_mfma_f32_16x16x32_bf16 v[32:35], v[168:171], v[208:211], v[32:35]\n\t" \
        "v_add_u32_e32 v225, s44, v225\n\t" \
        "v_mfma_f32_16x16x32_bf16 v[48:51], v[172:175], v[208:211], v[48:51]\n\t" \
        "v_add_u32_e32 v226, s44, v226\n\t" \
        "v_mfma_f32_16x16x32_bf16 v[64:67], v[176:179], v[208:211], v[64:67]\n\t" \
        "v_add_u32_e32 v227, s44, v227\n\t" \
        "v_mfma_f32_16x16x32_bf16 v[80:83], v[180:183], v[208:211], v[80:83]\n\t" \
        "v_add_u32_e32 v228, s44, v228\n\t" \
        "v_mfma_f32_16x16x32_bf16 v[96:99], v[184:187], v[208:211], v[96:99]\n\t" \
        "ds_read_b64_tr_b16 v[192:193], v225 offset:16384\n\t" \
        "v_mfma_f32_16x16x32_bf16 v[112:115], v[188:191], v[208:211], v[112:115]\n\t" \
        "v_mfma_f32_16x16x32_bf16 v[4:7], v[160:163], v[212:215], v[4:7]\n\t" \
        "ds_read_b64_tr_b16 v[194:195], v225 offset:18432\n\t" \
        "v_mfma_f32_16x16x32_bf16 v[20:23], v[164:167], v[212:215], v[20:23]\n\t" \
        "ds_read_b128 v[128:131], v224 offset:0\n\t" \
        "v_mfma_f32_16x16x32_bf16 v[36:39], v[168:171], v[212:215], v[36:39]\n\t" \
        "ds_read_b128 v[132:135], v224 offset:2048\n\t" \
        "v_mfma_f32_16x16x32_bf16 v[52:55], v[172:175], v[212:215], v[52:55]\n\t" \
        "ds_read_b128 v[136:139], v224 offset:4096\n\t" \
        "v_mfma_f32_16x16x32_bf16 v[68:71], v[176:179], v[212:215], v[68:71]\n\t" \
        "ds_read_b128 v[140:143], v224 offset:6144\n\t" \
        "v_mfma_f32_16x16x32_bf16 v[84:87], v[180:183], v[212:215], v[84:87]\n\t" \
        "ds_read_b128 v[144:147], v224 offset:8192\n\t" \
        "v_mfma_f32_16x16x32_bf16 v[100:103], v[184:187], v[212:215], v[100:103]\n\t" \
        "ds_read_b128 v[148:151], v224 offset:10240\n\t" \
        "v_mfma_f32_16x16x32_bf16 v[116:119], v[188:191], v[212:215], v[116:119]\n\t" \
        "v_mfma_f32_16x16x32_bf16 v[8:11], v[160:163], v[216:219], v[8:11]\n\t" \
        "ds_read_b128 v[152:155], v224 offset:12288\n\t" \
        "v_mfma_f32_16x16x32_bf16 v[24:27], v[164:167], v[216:219], v[24:27]\n\t" \
        "ds_read_b128 v[156:159], v224 offset:14336\n\t" \
        "v_mfma_f32_16x16x32_bf16 v[40:43], v[168:171], v[216:219], v[40:43]\n\t" \
        "ds_read_b64_tr_b16 v[196:197], v226 offset:16384\n\t" \
        "v_mfma_f32_16x16x32_bf16 v[56:59], v[172:175], v[216:219], v[56:59]\n\t" \
        "ds_read_b64_tr_b16 v[198:199], v226 offset:18432\n\t" \
        "v_mfma_f32_16x16x32_bf16 v[72:75], v[176:179], v[216:219], v[72:75]\n\t" \
        "ds_read_b64_tr_b16 v[200:201], v227 offset:16384\n\t" \
        "v_mfma_f32_16x16x32_bf16 v[88:91], v[180:183], v[216:219], v[88:91]\n\t" \
        "ds_read_b64_tr_b16 v[202:203], v227 offset:18432\n\t" \
        "v_mfma_f32_16x16x32_bf16 v[104:107], v[184:187], v[216:219], v[104:107]\n\t" \
        "ds_read_b64_tr_b16 v[204:205], v228 offset:16384\n\t" \
        "v_mfma_f32_16x16x32_bf16 v[120:123], v[188:191], v[216:219], v[120:123]\n\t" \
        "v_mfma_f32_16x16x32_bf16 v[12:15], v[160:163], v[220:223], v[12:15]\n\t" \
        "ds_read_b64_tr_b16 v[206:207], v228 offset:18432\n\t" \
        "v_mfma_f32_16x16x32_bf16 v[28:31], v[164:167], v[220:223], v[28:31]\n\t" \
        "v_mfma_f32_16x16x32_bf16 v[44:47], v[168:171], v[220:223], v[44:47]\n\t" \
        "v_mfma_f32_16x16x32_bf16 v[60:63], v[172:175], v[220:223], v[60:63]\n\t" \
        "v_mfma_f32_16x16x32_bf16 v[76:79], v[176:179], v[220:223], v[76:79]\n\t" \
        "v_mfma_f32_16x16x32_bf16 v[92:95], v[180:183], v[220:223], v[92:95]\n\t" \
        "v_mfma_f32_16x16x32_bf16 v[108:111], v[184:187], v[220:223], v[108:111]\n\t" \
        "v_mfma_f32_16x16x32_bf16 v[124:127], v[188:191], v[220:223], v[124:127]\n\t" \
        "s_waitcnt lgkmcnt(0)\n\t" \
        "v_mfma_f32_16x16x32_bf16 v[0:3], v[128:131], v[192:195], v[0:3]\n\t" \
        "v_xor_b32_e32 v224, 64, v224\n\t" \
        "v_mfma_f32_16x16x32_bf16 v[16:19], v[132:135], v[192:195], v[16:19]\n\t" \
        "ds_read_b64_tr_b16 v[208:209], v225 offset:32768\n\t" \
        "v_mfma_f32_16x16x32_bf16 v[32:35], v[136:139], v[192:195], v[32:35]\n\t" \
        "v_mfma_f32_16x16x32_bf16 v[48:51], v[140:143], v[192:195], v[48:51]\n\t" \
        "ds_read_b64_tr_b16 v[210:211], v225 offset:34816\n\t" \
        "v_mfma_f32_16x16x32_bf16 v[64:67], v[144:147], v[192:195], v[64:67]\n\t" \
        "ds_read_b128 v[160:163], v224 offset:0\n\t" \
        "v_mfma_f32_16x16x32_bf16 v[80:83], v[148:151], v[192:195], v[80:83]\n\t" \
        "v_mfma_f32_16x16x32_bf16 v[96:99], v[152:155], v[192:195], v[96:99]\n\t" \
        "ds_read_b128 v[164:167], v224 offset:2048\n\t" \
        "v_mfma_f32_16x16x32_bf16 v[112:115], v[156:159], v[192:195], v[112:115]\n\t" \
        "ds_read_b128 v[168:171], v224 offset:4096\n\t" \
        "v_mfma_f32_16x16x32_bf16 v[4:7], v[128:131], v[196:199], v[4:7]\n\t" \
        "v_mfma_f32_16x16x32_bf16 v[20:23], v[132:135], v[196:199], v[20:23]\n\t" \
        "ds_read_b128 v[172:175], v224 offset:6144\n\t" \
        "v_mfma_f32_16x16x32_bf16 v[36:39], v[136:139], v[196:199], v[36:39]\n\t" \
        "ds_read_b128 v[176:179], v224 offset:8192\n\t" \
        "v_mfma_f32_16x16x32_bf16 v[52:55], v[140:143], v[196:199], v[52:55]\n\t" \
        "v_mfma_f32_16x16x32_bf16 v[68:71], v[144:147], v[196:199], v[68:71]\n\t" \
        "ds_read_b128 v[180:183], v224 offset:10240\n\t" \
        "v_mfma_f32_16x16x32_bf16 v[84:87], v[148:151], v[196:199], v[84:87]\n\t" \
        "ds_read_b128 v[184:187], v224 offset:12288\n\t" \
        "v_mfma_f32_16x16x32_bf16 v[100:103], v[152:155], v[196:199], v[100:103]\n\t" \
        "v_mfma_f32_16x16x32_bf16 v[116:119], v[156:159], v[196:199], v[116:119]\n\t" \
        "ds_read_b128 v[188:191], v224 offset:14336\n\t" \
        "v_mfma_f32_16x16x32_bf16 v[8:11], v[128:131], v[200:203], v[8:11]\n\t" \
        "ds_read_b64_tr_b16 v[212:213], v226 offset:32768\n\t" \
        "v_mfma_f32_16x16x32_bf16 v[24:27], v[132:135], v[200:203], v[24:27]\n\t" \
        "v_mfma_f32_16x16x32_bf16 v[40:43], v[136:139], v[200:203], v[40:43]\n\t" \
        "ds_read_b64_tr_b16 v[214:215], v226 offset:34816\n\t" \
        "v_mfma_f32_16x16x32_bf16 v[56:59], v[140:143], v[200:203], v[56:59]\n\t" \
        "ds_read_b64_tr_b16 v[216:217], v227 offset:32768\n\t" \
        "v_mfma_f32_16x16x32_bf16 v[72:75], v[144:147], v[200:203], v[72:75]\n\t" \
        "v_mfma_f32_16x16x32_bf16 v[88:91], v[148:151], v[200:203], v[88:91]\n\t" \
        "ds_read_b64_tr_b16 v[218:219], v227 offset:34816\n\t" \
        "v_mfma_f32_16x16x32_bf16 v[104:107], v[152:155], v[200:203], v[104:107]\n\t" \
        "ds_read_b64_tr_b16 v[220:221], v228 offset:32768\n\t" \
        "v_mfma_f32_16x16x32_bf16 v[120:123], v[156:159], v[200:203], v[120:123]\n\t" \
        "v_mfma_f32_16x16x32_bf16 v[12:15], v[128:131], v[204:207], v[12:15]\n\t" \
        "ds_read_b64_tr_b16 v[222:223], v228 offset:34816\n\t" \
        "v_mfma_f32_16x16x32_bf16 v[28:31], v[132:135], v[204:207], v[28:31]\n\t" \
        "v_mfma_f32_16x16x32_bf16 v[44:47], v[136:139], v[204:207], v[44:47]\n\t" \
        "s_add_u32 s43, s43, 1\n\t" \
        "v_mfma_f32_16x16x32_bf16 v[60:63], v[140:143], v[204:207], v[60:63]\n\t" \
        "s_cmp_eq_u32 s43, 3\n\t" \
        "v_mfma_f32_16x16x32_bf16 v[76:79], v[144:147], v[204:207], v[76:79]\n\t" \
        "s_cselect_b32 s44, s46, s45\n\t" \
        "v_mfma_f32_16x16x32_bf16 v[92:95], v[148:151], v[204:207], v[92:95]\n\t" \
        "s_cselect_b32 s43, 0, s43\n\t" \
        "v_mfma_f32_16x16x32_bf16 v[108:111], v[152:155], v[204:207], v[108:111]\n\t" \
        "v_mfma_f32_16x16x32_bf16 v[124:127], v[156:159], v[204:207], v[124:127]\n\t" \
        "s_waitcnt lgkmcnt(0)\n\t" \
        "s_barrier\n\t" \
        "v_mfma_f32_16x16x32_bf16 v[0:3], v[160:163], v[208:211], v[0:3]\n\t" \
        "v_xor_b32_e32 v224, 64, v224\n\t" \
        "v_mfma_f32_16x16x32_bf16 v[16:19], v[164:167], v[208:211], v[16:19]\n\t" \
        "v_add_u32_e32 v224, s44, v224\n\t" \
        "v_mfma_f32_16x16x32_bf16 v[32:35], v[168:171], v[208:211], v[32:35]\n\t" \
        "v_add_u32_e32 v225, s44, v225\n\t" \
        "v_mfma_f32_16x16x32_bf16 v[48:51], v[172:175], v[208:211], v[48:51]\n\t" \
        "v_add_u32_e32 v226, s44, v226\n\t" \
        "v_mfma_f32_16x16x32_bf16 v[64:67], v[176:179], v[208:211], v[64:67]\n\t" \
        "v_add_u32_e32 v227, s44, v227\n\t" \
        "v_mfma_f32_16x16x32_bf16 v[80:83], v[180:183], v[208:211], v[80:83]\n\t" \
        "v_add_u32_e32 v228, s44, v228\n\t" \
        "v_mfma_f32_16x16x32_bf16 v[96:99], v[184:187], v[208:211], v[96:99]\n\t" \
        "ds_read_b64_tr_b16 v[192:193], v225 offset:16384\n\t" \
        "v_mfma_f32_16x16x32_bf16 v[112:115], v[188:191], v[208:211], v[112:115]\n\t" \
        "v_mfma_f32_16x16x32_bf16 v[4:7], v[160:163], v[212:215], v[4:7]\n\t" \
        "ds_read_b64_tr_b16 v[194:195], v225 offset:18432\n\t" \
        "v_mfma_f32_16x16x32_bf16 v[20:23], v[164:167], v[212:215], v[20:23]\n\t" \
        "ds_read_b128 v[128:131], v224 offset:0\n\t" \
        "v_mfma_f32_16x16x32_bf16 v[36:39], v[168:171], v[212:215], v[36:39]\n\t" \
        "ds_read_b128 v[132:135], v224 offset:2048\n\t" \
        "v_mfma_f32_16x16x32_bf16 v[52:55], v[172:175], v[212:215], v[52:55]\n\t" \
        "ds_read_b128 v[136:139], v224 offset:4096\n\t" \
        "v_mfma_f32_16x16x32_bf16 v[68:71], v[176:179], v[212:215], v[68:71]\n\t" \
        "ds_read_b128 v[140:143], v224 offset:6144\n\t" \
        "v_mfma_f32_16x16x32_bf16 v[84:87], v[180:183], v[212:215], v[84:87]\n\t" \
        "ds_read_b128 v[144:147], v224 offset:8192\n\t" \
        "v_mfma_f32_16x16x32_bf16 v[100:103], v[184:187], v[212:215], v[100:103]\n\t" \
        "ds_read_b128 v[148:151], v224 offset:10240\n\t" \
        "v_mfma_f32_16x16x32_bf16 v[116:119], v[188:191], v[212:215], v[116:119]\n\t" \
        "v_mfma_f32_16x16x32_bf16 v[8:11], v[160:163], v[216:219], v[8:11]\n\t" \
        "ds_read_b128 v[152:155], v224 offset:12288\n\t" \
        "v_mfma_f32_16x16x32_bf16 v[24:27], v[164:167], v[216:219], v[24:27]\n\t" \
        "ds_read_b128 v[156:159], v224 offset:14336\n\t" \
        "v_mfma_f32_16x16x32_bf16 v[40:43], v[168:171], v[216:219], v[40:43]\n\t" \
        "ds_read_b64_tr_b16 v[196:197], v226 offset:16384\n\t" \
        "v_mfma_f32_16x16x32_bf16 v[56:59], v[172:175], v[216:219], v[56:59]\n\t" \
        "ds_read_b64_tr_b16 v[198:199], v226 offset:18432\n\t" \
        "v_mfma_f32_16x16x32_bf16 v[72:75], v[176:179], v[216:219], v[72:75]\n\t" \
        "ds_read_b64_tr_b16 v[200:201], v227 offset:16384\n\t" \
        "v_mfma_f32_16x16x32_bf16 v[88:91], v[180:183], v[216:219], v[88:91]\n\t" \
        "ds_read_b64_tr_b16 v[202:203], v227 offset:18432\n\t" \
        "v_mfma_f32_16x16x32_bf16 v[104:107], v[184:187], v[216:219], v[104:107]\n\t" \
        "ds_read_b64_tr_b16 v[204:205], v228 offset:16384\n\t" \
        "v_mfma_f32_16x16x32_bf16 v[120:123], v[188:191], v[216:219], v[120:123]\n\t" \
        "v_mfma_f32_16x16x32_bf16 v[12:15], v[160:163], v[220:223], v[12:15]\n\t" \
        "ds_read_b64_tr_b16 v[206:207], v228 offset:18432\n\t" \
        "v_mfma_f32_16x16x32_bf16 v[28:31], v[164:167], v[220:223], v[28:31]\n\t" \
        "v_mfma_f32_16x16x32_bf16 v[44:47], v[168:171], v[220:223], v[44:47]\n\t" \
        "v_mfma_f32_16x16x32_bf16 v[60:63], v[172:175], v[220:223], v[60:63]\n\t" \
        "v_mfma_f32_16x16x32_bf16 v[76:79], v[176:179], v[220:223], v[76:79]\n\t" \
        "v_mfma_f32_16x16x32_bf16 v[92:95], v[180:183], v[220:223], v[92:95]\n\t" \
        "v_mfma_f32_16x16x32_bf16 v[108:111], v[184:187], v[220:223], v[108:111]\n\t" \
        "v_mfma_f32_16x16x32_bf16 v[124:127], v[188:191], v[220:223], v[124:127]\n\t" \
        "s_add_u32 s42, s42, 2\n\t" \
        "s_cmp_lt_u32 s42, 16\n\t" \
        "s_cbranch_scc1 1b\n\t" \
        "s_waitcnt lgkmcnt(0)\n\t" \
        "s_nop 7\n\t" \
        "s_nop 1\n\t"
#define PROJ_V_CLOBBERS "v128", "v129", "v130", "v131", "v132", "v133", "v134", "v135", "v136", "v137", "v138", "v139", "v140", "v141", "v142", "v143", "v144", "v145", "v146", "v147", "v148", "v149", "v150", "v151", "v152", "v153", "v154", "v155", "v156", "v157", "v158", "v159", "v160", "v161", "v162", "v163", "v164", "v165", "v166", "v167", "v168", "v169", "v170", "v171", "v172", "v173", "v174", "v175", "v176", "v177", "v178", "v179", "v180", "v181", "v182", "v183", "v184", "v185", "v186", "v187", "v188", "v189", "v190", "v191", "v192", "v193", "v194", "v195", "v196", "v197", "v198", "v199", "v200", "v201", "v202", "v203", "v204", "v205", "v206", "v207", "v208", "v209", "v210", "v211", "v212", "v213", "v214", "v215", "v216", "v217", "v218", "v219", "v220", "v221", "v222", "v223", "s42", "s43", "s44", "s45", "s46"
#define PROJ_QS_ASM \
        "ds_read_b64_tr_b16 v[192:193], v228 offset:16384\n\t" \
        "ds_read_b64_tr_b16 v[194:195], v228 offset:18432\n\t" \
        "ds_read_b64_tr_b16 v[240:241], v244 offset:49152\n\t" \
        "ds_read_b64_tr_b16 v[242:243], v244 offset:49280\n\t" \
        "ds_read_b128 v[128:131], v224 offset:0\n\t" \
        "ds_read_b128 v[132:135], v224 offset:2048\n\t" \
        "ds_read_b128 v[136:139], v225 offset:0\n\t" \
        "ds_read_b128 v[140:143], v225 offset:2048\n\t" \
        "ds_read_b128 v[144:147], v226 offset:0\n\t" \
        "ds_read_b128 v[148:151], v226 offset:2048\n\t" \
        "ds_read_b128 v[152:155], v227 offset:0\n\t" \
        "ds_read_b128 v[156:159], v227 offset:2048\n\t" \
        "ds_read_b64_tr_b16 v[196:197], v229 offset:16384\n\t" \
        "ds_read_b64_tr_b16 v[198:199], v229 offset:18432\n\t" \
        "ds_read_b64_tr_b16 v[200:201], v230 offset:16384\n\t" \
        "ds_read_b64_tr_b16 v[202:203], v230 offset:18432\n\t" \
        "ds_read_b64_tr_b16 v[204:205], v231 offset:16384\n\t" \
        "ds_read_b64_tr_b16 v[206:207], v231 offset:18432\n\t" \
        "s_mov_b32 s42, 0\n\t" \
        "s_mov_b32 s43, 0\n\t" \
        "s_mov_b32 s45, 51200\n\t" \
        "s_mov_b32 s46, -102400\n\t" \
        "1:\n\t" \
        "s_waitcnt lgkmcnt(0)\n\t" \
        "v_mfma_f32_16x16x32_bf16 v[232:235], v[240:243], v[128:131], v[232:235]\n\t" \
        "v_mfma_f32_16x16x32_bf16 v[236:239], v[240:243], v[132:135], v[236:239]\n\t" \
        "v_mfma_f32_16x16x32_bf16 v[0:3], v[192:195], v[128:131], v[0:3]\n\t" \
        "v_xor_b32_e32 v224, 64, v224\n\t" \
        "v_mfma_f32_16x16x32_bf16 v[16:19], v[192:195], v[132:135], v[16:19]\n\t" \
        "v_xor_b32_e32 v225, 64, v225\n\t" \
        "v_mfma_f32_16x16x32_bf16 v[32:35], v[192:195], v[136:139], v[32:35]\n\t" \
        "v_xor_b32_e32 v226, 64, v226\n\t" \
        "v_mfma_f32_16x16x32_bf16 v[48:51], v[192:195], v[140:143], v[48:51]\n\t" \
        "v_xor_b32_e32 v227, 64, v227\n\t" \
        "v_mfma_f32_16x16x32_bf16 v[64:67], v[192:195], v[144:147], v[64:67]\n\t" \
        "ds_read_b64_tr_b16 v[208:209], v228 offset:32768\n\t" \
        "v_mfma_f32_16x16x32_bf16 v[80:83], v[192:195], v[148:151], v[80:83]\n\t" \
        "ds_read_b64_tr_b16 v[210:211], v228 offset:34816\n\t" \
        "v_mfma_f32_16x16x32_bf16 v[96:99], v[192:195], v[152:155], v[96:99]\n\t" \
        "ds_read_b64_tr_b16 v[240:241], v244 offset:50176\n\t" \
        "v_mfma_f32_16x16x32_bf16 v[112:115], v[192:195], v[156:159], v[112:115]\n\t" \
        "v_mfma_f32_16x16x32_bf16 v[4:7], v[196:199], v[128:131], v[4:7]\n\t" \
        "ds_read_b64_tr_b16 v[242:243], v244 offset:50304\n\t" \
        "v_mfma_f32_16x16x32_bf16 v[20:23], v[196:199], v[132:135], v[20:23]\n\t" \
        "ds_read_b128 v[160:163], v224 offset:0\n\t" \
        "v_mfma_f32_16x16x32_bf16 v[36:39], v[196:199], v[136:139], v[36:39]\n\t" \
        "ds_read_b128 v[164:167], v224 offset:2048\n\t" \
        "v_mfma_f32_16x16x32_bf16 v[52:55], v[196:199], v[140:143], v[52:55]\n\t" \
        "ds_read_b128 v[168:171], v225 offset:0\n\t" \
        "v_mfma_f32_16x16x32_bf16 v[68:71], v[196:199], v[144:147], v[68:71]\n\t" \
        "ds_read_b128 v[172:175], v225 offset:2048\n\t" \
        "v_mfma_f32_16x16x32_bf16 v[84:87], v[196:199], v[148:151], v[84:87]\n\t" \
        "ds_read_b128 v[176:179], v226 offset:0\n\t" \
        "v_mfma_f32_16x16x32_bf16 v[100:103], v[196:199], v[152:155], v[100:103]\n\t" \
        "ds_read_b128 v[180:183], v226 offset:2048\n\t" \
        "v_mfma_f32_16x16x32_bf16 v[116:119], v[196:199], v[156:159], v[116:119]\n\t" \
        "v_mfma_f32_16x16x32_bf16 v[8:11], v[200:203], v[128:131], v[8:11]\n\t" \
        "ds_read_b128 v[184:187], v227 offset:0\n\t" \
        "v_mfma_f32_16x16x32_bf16 v[24:27], v[200:203], v[132:135], v[24:27]\n\t" \
        "ds_read_b128 v[188:191], v227 offset:2048\n\t" \
        "v_mfma_f32_16x16x32_bf16 v[40:43], v[200:203], v[136:139], v[40:43]\n\t" \
        "ds_read_b64_tr_b16 v[212:213], v229 offset:32768\n\t" \
        "v_mfma_f32_16x16x32_bf16 v[56:59], v[200:203], v[140:143], v[56:59]\n\t" \
        "ds_read_b64_tr_b16 v[214:215], v229 offset:34816\n\t" \
        "v_mfma_f32_16x16x32_bf16 v[72:75], v[200:203], v[144:147], v[72:75]\n\t" \
        "ds_read_b64_tr_b16 v[216:217], v230 offset:32768\n\t" \
        "v_mfma_f32_16x16x32_bf16 v[88:91], v[200:203], v[148:151], v[88:91]\n\t" \
        "ds_read_b64_tr_b16 v[218:219], v230 offset:34816\n\t" \
        "v_mfma_f32_16x16x32_bf16 v[104:107], v[200:203], v[152:155], v[104:107]\n\t" \
        "ds_read_b64_tr_b16 v[220:221], v231 offset:32768\n\t" \
        "v_mfma_f32_16x16x32_bf16 v[120:123], v[200:203], v[156:159], v[120:123]\n\t" \
        "v_mfma_f32_16x16x32_bf16 v[12:15], v[204:207], v[128:131], v[12:15]\n\t" \
        "ds_read_b64_tr_b16 v[222:223], v231 offset:34816\n\t" \
        "v_mfma_f32_16x16x32_bf16 v[28:31], v[204:207], v[132:135], v[28:31]\n\t" \
        "v_mfma_f32_16x16x32_bf16 v[44:47], v[204:207], v[136:139], v[44:47]\n\t" \
        "s_add_u32 s43, s43, 1\n\t" \
        "v_mfma_f32_16x16x32_bf16 v[60:63], v[204:207], v[140:143], v[60:63]\n\t" \
        "s_cmp_eq_u32 s43, 3\n\t" \
        "v_mfma_f32_16x16x32_bf16 v[76:79], v[204:207], v[144:147], v[76:79]\n\t" \
        "s_cselect_b32 s44, s46, s45\n\t" \
        "v_mfma_f32_16x16x32_bf16 v[92:95], v[204:207], v[148:151], v[92:95]\n\t" \
        "s_cselect_b32 s43, 0, s43\n\t" \
        "v_mfma_f32_16x16x32_bf16 v[108:111], v[204:207], v[152:155], v[108:111]\n\t" \
        "v_mfma_f32_16x16x32_bf16 v[124:127], v[204:207], v[156:159], v[124:127]\n\t" \
        "s_waitcnt lgkmcnt(0)\n\t" \
        "s_barrier\n\t" \
        "v_mfma_f32_16x16x32_bf16 v[232:235], v[240:243], v[160:163], v[232:235]\n\t" \
        "v_mfma_f32_16x16x32_bf16 v[236:239], v[240:243], v[164:167], v[236:239]\n\t" \
        "v_mfma_f32_16x16x32_bf16 v[0:3], v[208:211], v[160:163], v[0:3]\n\t" \
        "v_xor_b32_e32 v224, 64, v224\n\t" \
        "v_xor_b32_e32 v225, 64, v225\n\t" \
        "v_mfma_f32_16x16x32_bf16 v[16:19], v[208:211], v[164:167], v[16:19]\n\t" \
        "v_xor_b32_e32 v226, 64, v226\n\t" \
        "v_mfma_f32_16x16x32_bf16 v[32:35], v[208:211], v[168:171], v[32:35]\n\t" \
        "v_xor_b32_e32 v227, 64, v227\n\t" \
        "v_mfma_f32_16x16x32_bf16 v[48:51], v[208:211], v[172:175], v[48:51]\n\t" \
        "v_add_u32_e32 v224, s44, v224\n\t" \
        "v_mfma_f32_16x16x32_bf16 v[64:67], v[208:211], v[176:179], v[64:67]\n\t" \
        "v_add_u32_e32 v225, s44, v225\n\t" \
        "v_add_u32_e32 v226, s44, v226\n\t" \
        "v_mfma_f32_16x16x32_bf16 v[80:83], v[208:211], v[180:183], v[80:83]\n\t" \
        "v_add_u32_e32 v227, s44, v227\n\t" \
        "v_mfma_f32_16x16x32_bf16 v[96:99], v[208:211], v[184:187], v[96:99]\n\t" \
        "v_add_u32_e32 v228, s44, v228\n\t" \
        "v_mfma_f32_16x16x32_bf16 v[112:115], v[208:211], v[188:191], v[112:115]\n\t" \
        "v_add_u32_e32 v229, s44, v229\n\t" \
        "v_mfma_f32_16x16x32_bf16 v[4:7], v[212:215], v[160:163], v[4:7]\n\t" \
        "v_add_u32_e32 v230, s44, v230\n\t" \
        "v_add_u32_e32 v231, s44, v231\n\t" \
        "v_mfma_f32_16x16x32_bf16 v[20:23], v[212:215], v[164:167], v[20:23]\n\t" \
        "v_add_u32_e32 v244, s44, v244\n\t" \
        "v_mfma_f32_16x16x32_bf16 v[36:39], v[212:215], v[168:171], v[36:39]\n\t" \
        "ds_read_b64_tr_b16 v[192:193], v228 offset:16384\n\t" \
        "v_mfma_f32_16x16x32_bf16 v[52:55], v[212:215], v[172:175], v[52:55]\n\t" \
        "ds_read_b64_tr_b16 v[194:195], v228 offset:18432\n\t" \
        "v_mfma_f32_16x16x32_bf16 v[68:71], v[212:215], v[176:179], v[68:71]\n\t" \
        "ds_read_b64_tr_b16 v[240:241], v244 offset:49152\n\t" \
        "ds_read_b64_tr_b16 v[242:243], v244 offset:49280\n\t" \
        "v_mfma_f32_16x16x32_bf16 v[84:87], v[212:215], v[180:183], v[84:87]\n\t" \
        "ds_read_b128 v[128:131], v224 offset:0\n\t" \
        "v_mfma_f32_16x16x32_bf16 v[100:103], v[212:215], v[184:187], v[100:103]\n\t" \
        "ds_read_b128 v[132:135], v224 offset:2048\n\t" \
        "v_mfma_f32_16x16x32_bf16 v[116:119], v[212:215], v[188:191], v[116:119]\n\t" \
        "ds_read_b128 v[136:139], v225 offset:0\n\t" \
        "v_mfma_f32_16x16x32_bf16 v[8:11], v[216:219], v[160:163], v[8:11]\n\t" \
        "ds_read_b128 v[140:143], v225 offset:2048\n\t" \
        "ds_read_b128 v[144:147], v226 offset:0\n\t" \
        "v_mfma_f32_16x16x32_bf16 v[24:27], v[216:219], v[164:167], v[24:27]\n\t" \
        "ds_read_b128 v[148:151], v226 offset:2048\n\t" \
        "v_mfma_f32_16x16x32_bf16 v[40:43], v[216:219], v[168:171], v[40:43]\n\t" \
        "ds_read_b128 v[152:155], v227 offset:0\n\t" \
        "v_mfma_f32_16x16x32_bf16 v[56:59], v[216:219], v[172:175], v[56:59]\n\t" \
        "ds_read_b128 v[156:159], v227 offset:2048\n\t" \
        "v_mfma_f32_16x16x32_bf16 v[72:75], v[216:219], v[176:179], v[72:75]\n\t" \
        "ds_read_b64_tr_b16 v[196:197], v229 offset:16384\n\t" \
        "ds_read_b64_tr_b16 v[198:199], v229 offset:18432\n\t" \
        "v_mfma_f32_16x16x32_bf16 v[88:91], v[216:219], v[180:183], v[88:91]\n\t" \
        "ds_read_b64_tr_b16 v[200:201], v230 offset:16384\n\t" \
        "v_mfma_f32_16x16x32_bf16 v[104:107], v[216:219], v[184:187], v[104:107]\n\t" \
        "ds_read_b64_tr_b16 v[202:203], v230 offset:18432\n\t" \
        "v_mfma_f32_16x16x32_bf16 v[120:123], v[216:219], v[188:191], v[120:123]\n\t" \
        "ds_read_b64_tr_b16 v[204:205], v231 offset:16384\n\t" \
        "v_mfma_f32_16x16x32_bf16 v[12:15], v[220:223], v[160:163], v[12:15]\n\t" \
        "ds_read_b64_tr_b16 v[206:207], v231 offset:18432\n\t" \
        "v_mfma_f32_16x16x32_bf16 v[28:31], v[220:223], v[164:167], v[28:31]\n\t" \
        "v_mfma_f32_16x16x32_bf16 v[44:47], v[220:223], v[168:171], v[44:47]\n\t" \
        "v_mfma_f32_16x16x32_bf16 v[60:63], v[220:223], v[172:175], v[60:63]\n\t" \
        "v_mfma_f32_16x16x32_bf16 v[76:79], v[220:223], v[176:179], v[76:79]\n\t" \
        "v_mfma_f32_16x16x32_bf16 v[92:95], v[220:223], v[180:183], v[92:95]\n\t" \
        "v_mfma_f32_16x16x32_bf16 v[108:111], v[220:223], v[184:187], v[108:111]\n\t" \
        "v_mfma_f32_16x16x32_bf16 v[124:127], v[220:223], v[188:191], v[124:127]\n\t" \
        "s_waitcnt lgkmcnt(0)\n\t" \
        "v_mfma_f32_16x16x32_bf16 v[232:235], v[240:243], v[128:131], v[232:235]\n\t" \
        "v_mfma_f32_16x16x32_bf16 v[236:239], v[240:243], v[132:135], v[236:239]\n\t" \
        "v_mfma_f32_16x16x32_bf16 v[0:3], v[192:195], v[128:131], v[0:3]\n\t" \
        "v_xor_b32_e32 v224, 64, v224\n\t" \
        "v_mfma_f32_16x16x32_bf16 v[16:19], v[192:195], v[132:135], v[16:19]\n\t" \
        "v_xor_b32_e32 v225, 64, v225\n\t" \
        "v_mfma_f32_16x16x32_bf16 v[32:35], v[192:195], v[136:139], v[32:35]\n\t" \
        "v_xor_b32_e32 v226, 64, v226\n\t" \
        "v_mfma_f32_16x16x32_bf16 v[48:51], v[192:195], v[140:143], v[48:51]\n\t" \
        "v_xor_b32_e32 v227, 64, v227\n\t" \
        "v_mfma_f32_16x16x32_bf16 v[64:67], v[192:195], v[144:147], v[64:67]\n\t" \
        "ds_read_b64_tr_b16 v[208:209], v228 offset:32768\n\t" \
        "v_mfma_f32_16x16x32_bf16 v[80:83], v[192:195], v[148:151], v[80:83]\n\t" \
        "ds_read_b64_tr_b16 v[210:211], v228 offset:34816\n\t" \
        "v_mfma_f32_16x16x32_bf16 v[96:99], v[192:195], v[152:155], v[96:99]\n\t" \
        "ds_read_b64_tr_b16 v[240:241], v244 offset:50176\n\t" \
        "v_mfma_f32_16x16x32_bf16 v[112:115], v[192:195], v[156:159], v[112:115]\n\t" \
        "v_mfma_f32_16x16x32_bf16 v[4:7], v[196:199], v[128:131], v[4:7]\n\t" \
        "ds_read_b64_tr_b16 v[242:243], v244 offset:50304\n\t" \
        "v_mfma_f32_16x16x32_bf16 v[20:23], v[196:199], v[132:135], v[20:23]\n\t" \
        "ds_read_b128 v[160:163], v224 offset:0\n\t" \
        "v_mfma_f32_16x16x32_bf16 v[36:39], v[196:199], v[136:139], v[36:39]\n\t" \
        "ds_read_b128 v[164:167], v224 offset:2048\n\t" \
        "v_mfma_f32_16x16x32_bf16 v[52:55], v[196:199], v[140:143], v[52:55]\n\t" \
        "ds_read_b128 v[168:171], v225 offset:0\n\t" \
        "v_mfma_f32_16x16x32_bf16 v[68:71], v[196:199], v[144:147], v[68:71]\n\t" \
        "ds_read_b128 v[172:175], v225 offset:2048\n\t" \
        "v_mfma_f32_16x16x32_bf16 v[84:87], v[196:199], v[148:151], v[84:87]\n\t" \
        "ds_read_b128 v[176:179], v226 offset:0\n\t" \
        "v_mfma_f32_16x16x32_bf16 v[100:103], v[196:199], v[152:155], v[100:103]\n\t" \
        "ds_read_b128 v[180:183], v226 offset:2048\n\t" \
        "v_mfma_f32_16x16x32_bf16 v[116:119], v[196:199], v[156:159], v[116:119]\n\t" \
        "v_mfma_f32_16x16x32_bf16 v[8:11], v[200:203], v[128:131], v[8:11]\n\t" \
        "ds_read_b128 v[184:187], v227 offset:0\n\t" \
        "v_mfma_f32_16x16x32_bf16 v[24:27], v[200:203], v[132:135], v[24:27]\n\t" \
        "ds_read_b128 v[188:191], v227 offset:2048\n\t" \
        "v_mfma_f32_16x16x32_bf16 v[40:43], v[200:203], v[136:139], v[40:43]\n\t" \
        "ds_read_b64_tr_b16 v[212:213], v229 offset:32768\n\t" \
        "v_mfma_f32_16x16x32_bf16 v[56:59], v[200:203], v[140:143], v[56:59]\n\t" \
        "ds_read_b64_tr_b16 v[214:215], v229 offset:34816\n\t" \
        "v_mfma_f32_16x16x32_bf16 v[72:75], v[200:203], v[144:147], v[72:75]\n\t" \
        "ds_read_b64_tr_b16 v[216:217], v230 offset:32768\n\t" \
        "v_mfma_f32_16x16x32_bf16 v[88:91], v[200:203], v[148:151], v[88:91]\n\t" \
        "ds_read_b64_tr_b16 v[218:219], v230 offset:34816\n\t" \
        "v_mfma_f32_16x16x32_bf16 v[104:107], v[200:203], v[152:155], v[104:107]\n\t" \
        "ds_read_b64_tr_b16 v[220:221], v231 offset:32768\n\t" \
        "v_mfma_f32_16x16x32_bf16 v[120:123], v[200:203], v[156:159], v[120:123]\n\t" \
        "v_mfma_f32_16x16x32_bf16 v[12:15], v[204:207], v[128:131], v[12:15]\n\t" \
        "ds_read_b64_tr_b16 v[222:223], v231 offset:34816\n\t" \
        "v_mfma_f32_16x16x32_bf16 v[28:31], v[204:207], v[132:135], v[28:31]\n\t" \
        "v_mfma_f32_16x16x32_bf16 v[44:47], v[204:207], v[136:139], v[44:47]\n\t" \
        "s_add_u32 s43, s43, 1\n\t" \
        "v_mfma_f32_16x16x32_bf16 v[60:63], v[204:207], v[140:143], v[60:63]\n\t" \
        "s_cmp_eq_u32 s43, 3\n\t" \
        "v_mfma_f32_16x16x32_bf16 v[76:79], v[204:207], v[144:147], v[76:79]\n\t" \
        "s_cselect_b32 s44, s46, s45\n\t" \
        "v_mfma_f32_16x16x32_bf16 v[92:95], v[204:207], v[148:151], v[92:95]\n\t" \
        "s_cselect_b32 s43, 0, s43\n\t" \
        "v_mfma_f32_16x16x32_bf16 v[108:111], v[204:207], v[152:155], v[108:111]\n\t" \
        "v_mfma_f32_16x16x32_bf16 v[124:127], v[204:207], v[156:159], v[124:127]\n\t" \
        "s_waitcnt lgkmcnt(0)\n\t" \
        "s_barrier\n\t" \
        "v_mfma_f32_16x16x32_bf16 v[232:235], v[240:243], v[160:163], v[232:235]\n\t" \
        "v_mfma_f32_16x16x32_bf16 v[236:239], v[240:243], v[164:167], v[236:239]\n\t" \
        "v_mfma_f32_16x16x32_bf16 v[0:3], v[208:211], v[160:163], v[0:3]\n\t" \
        "v_xor_b32_e32 v224, 64, v224\n\t" \
        "v_xor_b32_e32 v225, 64, v225\n\t" \
        "v_mfma_f32_16x16x32_bf16 v[16:19], v[208:211], v[164:167], v[16:19]\n\t" \
        "v_xor_b32_e32 v226, 64, v226\n\t" \
        "v_mfma_f32_16x16x32_bf16 v[32:35], v[208:211], v[168:171], v[32:35]\n\t" \
        "v_xor_b32_e32 v227, 64, v227\n\t" \
        "v_mfma_f32_16x16x32_bf16 v[48:51], v[208:211], v[172:175], v[48:51]\n\t" \
        "v_add_u32_e32 v224, s44, v224\n\t" \
        "v_mfma_f32_16x16x32_bf16 v[64:67], v[208:211], v[176:179], v[64:67]\n\t" \
        "v_add_u32_e32 v225, s44, v225\n\t" \
        "v_add_u32_e32 v226, s44, v226\n\t" \
        "v_mfma_f32_16x16x32_bf16 v[80:83], v[208:211], v[180:183], v[80:83]\n\t" \
        "v_add_u32_e32 v227, s44, v227\n\t" \
        "v_mfma_f32_16x16x32_bf16 v[96:99], v[208:211], v[184:187], v[96:99]\n\t" \
        "v_add_u32_e32 v228, s44, v228\n\t" \
        "v_mfma_f32_16x16x32_bf16 v[112:115], v[208:211], v[188:191], v[112:115]\n\t" \
        "v_add_u32_e32 v229, s44, v229\n\t" \
        "v_mfma_f32_16x16x32_bf16 v[4:7], v[212:215], v[160:163], v[4:7]\n\t" \
        "v_add_u32_e32 v230, s44, v230\n\t" \
        "v_add_u32_e32 v231, s44, v231\n\t" \
        "v_mfma_f32_16x16x32_bf16 v[20:23], v[212:215], v[164:167], v[20:23]\n\t" \
        "v_add_u32_e32 v244, s44, v244\n\t" \
        "v_mfma_f32_16x16x32_bf16 v[36:39], v[212:215], v[168:171], v[36:39]\n\t" \
        "ds_read_b64_tr_b16 v[192:193], v228 offset:16384\n\t" \
        "v_mfma_f32_16x16x32_bf16 v[52:55], v[212:215], v[172:175], v[52:55]\n\t" \
        "ds_read_b64_tr_b16 v[194:195], v228 offset:18432\n\t" \
        "v_mfma_f32_16x16x32_bf16 v[68:71], v[212:215], v[176:179], v[68:71]\n\t" \
        "ds_read_b64_tr_b16 v[240:241], v244 offset:49152\n\t" \
        "ds_read_b64_tr_b16 v[242:243], v244 offset:49280\n\t" \
        "v_mfma_f32_16x16x32_bf16 v[84:87], v[212:215], v[180:183], v[84:87]\n\t" \
        "ds_read_b128 v[128:131], v224 offset:0\n\t" \
        "v_mfma_f32_16x16x32_bf16 v[100:103], v[212:215], v[184:187], v[100:103]\n\t" \
        "ds_read_b128 v[132:135], v224 offset:2048\n\t" \
        "v_mfma_f32_16x16x32_bf16 v[116:119], v[212:215], v[188:191], v[116:119]\n\t" \
        "ds_read_b128 v[136:139], v225 offset:0\n\t" \
        "v_mfma_f32_16x16x32_bf16 v[8:11], v[216:219], v[160:163], v[8:11]\n\t" \
        "ds_read_b128 v[140:143], v225 offset:2048\n\t" \
        "ds_read_b128 v[144:147], v226 offset:0\n\t" \
        "v_mfma_f32_16x16x32_bf16 v[24:27], v[216:219], v[164:167], v[24:27]\n\t" \
        "ds_read_b128 v[148:151], v226 offset:2048\n\t" \
        "v_mfma_f32_16x16x32_bf16 v[40:43], v[216:219], v[168:171], v[40:43]\n\t" \
        "ds_read_b128 v[152:155], v227 offset:0\n\t" \
        "v_mfma_f32_16x16x32_bf16 v[56:59], v[216:219], v[172:175], v[56:59]\n\t" \
        "ds_read_b128 v[156:159], v227 offset:2048\n\t" \
        "v_mfma_f32_16x16x32_bf16 v[72:75], v[216:219], v[176:179], v[72:75]\n\t" \
        "ds_read_b64_tr_b16 v[196:197], v229 offset:16384\n\t" \
        "ds_read_b64_tr_b16 v[198:199], v229 offset:18432\n\t" \
        "v_mfma_f32_16x16x32_bf16 v[88:91], v[216:219], v[180:183], v[88:91]\n\t" \
        "ds_read_b64_tr_b16 v[200:201], v230 offset:16384\n\t" \
        "v_mfma_f32_16x16x32_bf16 v[104:107], v[216:219], v[184:187], v[104:107]\n\t" \
        "ds_read_b64_tr_b16 v[202:203], v230 offset:18432\n\t" \
        "v_mfma_f32_16x16x32_bf16 v[120:123], v[216:219], v[188:191], v[120:123]\n\t" \
        "ds_read_b64_tr_b16 v[204:205], v231 offset:16384\n\t" \
        "v_mfma_f32_16x16x32_bf16 v[12:15], v[220:223], v[160:163], v[12:15]\n\t" \
        "ds_read_b64_tr_b16 v[206:207], v231 offset:18432\n\t" \
        "v_mfma_f32_16x16x32_bf16 v[28:31], v[220:223], v[164:167], v[28:31]\n\t" \
        "v_mfma_f32_16x16x32_bf16 v[44:47], v[220:223], v[168:171], v[44:47]\n\t" \
        "v_mfma_f32_16x16x32_bf16 v[60:63], v[220:223], v[172:175], v[60:63]\n\t" \
        "v_mfma_f32_16x16x32_bf16 v[76:79], v[220:223], v[176:179], v[76:79]\n\t" \
        "v_mfma_f32_16x16x32_bf16 v[92:95], v[220:223], v[180:183], v[92:95]\n\t" \
        "v_mfma_f32_16x16x32_bf16 v[108:111], v[220:223], v[184:187], v[108:111]\n\t" \
        "v_mfma_f32_16x16x32_bf16 v[124:127], v[220:223], v[188:191], v[124:127]\n\t" \
        "s_add_u32 s42, s42, 2\n\t" \
        "s_cmp_lt_u32 s42, 16\n\t" \
        "s_cbranch_scc1 1b\n\t" \
        "s_waitcnt lgkmcnt(0)\n\t" \
        "s_nop 7\n\t" \
        "s_nop 1\n\t"
#define PROJ_QS_CLOBBERS "v128", "v129", "v130", "v131", "v132", "v133", "v134", "v135", "v136", "v137", "v138", "v139", "v140", "v141", "v142", "v143", "v144", "v145", "v146", "v147", "v148", "v149", "v150", "v151", "v152", "v153", "v154", "v155", "v156", "v157", "v158", "v159", "v160", "v161", "v162", "v163", "v164", "v165", "v166", "v167", "v168", "v169", "v170", "v171", "v172", "v173", "v174", "v175", "v176", "v177", "v178", "v179", "v180", "v181", "v182", "v183", "v184", "v185", "v186", "v187", "v188", "v189", "v190", "v191", "v192", "v193", "v194", "v195", "v196", "v197", "v198", "v199", "v200", "v201", "v202", "v203", "v204", "v205", "v206", "v207", "v208", "v209", "v210", "v211", "v212", "v213", "v214", "v215", "v216", "v217", "v218", "v219", "v220", "v221", "v222", "v223", "v240", "v241", "v242", "v243", "s42", "s43", "s44", "s45", "s46"
#define OPROJ_ASM \
        "ds_read_b64_tr_b16 v[96:97], v129 offset:16384\n\t" \
        "ds_read_b64_tr_b16 v[98:99], v129 offset:18432\n\t" \
        "ds_read_b128 v[64:67], v128 offset:0\n\t" \
        "ds_read_b128 v[68:71], v128 offset:2048\n\t" \
        "ds_read_b128 v[72:75], v128 offset:4096\n\t" \
        "ds_read_b128 v[76:79], v128 offset:6144\n\t" \
        "ds_read_b64_tr_b16 v[100:101], v130 offset:16384\n\t" \
        "ds_read_b64_tr_b16 v[102:103], v130 offset:18432\n\t" \
        "ds_read_b64_tr_b16 v[104:105], v131 offset:16384\n\t" \
        "ds_read_b64_tr_b16 v[106:107], v131 offset:18432\n\t" \
        "ds_read_b64_tr_b16 v[108:109], v132 offset:16384\n\t" \
        "ds_read_b64_tr_b16 v[110:111], v132 offset:18432\n\t" \
        "s_mov_b32 s42, 0\n\t" \
        "s_mov_b32 s43, 0\n\t" \
        "s_mov_b32 s45, 32768\n\t" \
        "s_mov_b32 s46, -98304\n\t" \
        "1:\n\t" \
        "s_waitcnt lgkmcnt(0)\n\t" \
        "v_mfma_f32_16x16x32_bf16 v[0:3], v[96:99], v[64:67], v[0:3]\n\t" \
        "ds_read_b64_tr_b16 v[112:113], v129 offset:24576\n\t" \
        "v_mfma_f32_16x16x32_bf16 v[16:19], v[96:99], v[68:71], v[16:19]\n\t" \
        "ds_read_b64_tr_b16 v[114:115], v129 offset:26624\n\t" \
        "v_mfma_f32_16x16x32_bf16 v[32:35], v[96:99], v[72:75], v[32:35]\n\t" \
        "ds_read_b128 v[80:83], v128 offset:1024\n\t" \
        "v_mfma_f32_16x16x32_bf16 v[48:51], v[96:99], v[76:79], v[48:51]\n\t" \
        "ds_read_b128 v[84:87], v128 offset:3072\n\t" \
        "v_mfma_f32_16x16x32_bf16 v[4:7], v[100:103], v[64:67], v[4:7]\n\t" \
        "ds_read_b128 v[88:91], v128 offset:5120\n\t" \
        "v_mfma_f32_16x16x32_bf16 v[20:23], v[100:103], v[68:71], v[20:23]\n\t" \
        "ds_read_b128 v[92:95], v128 offset:7168\n\t" \
        "v_mfma_f32_16x16x32_bf16 v[36:39], v[100:103], v[72:75], v[36:39]\n\t" \
        "ds_read_b64_tr_b16 v[116:117], v130 offset:24576\n\t" \
        "v_mfma_f32_16x16x32_bf16 v[52:55], v[100:103], v[76:79], v[52:55]\n\t" \
        "ds_read_b64_tr_b16 v[118:119], v130 offset:26624\n\t" \
        "v_mfma_f32_16x16x32_bf16 v[8:11], v[104:107], v[64:67], v[8:11]\n\t" \
        "ds_read_b64_tr_b16 v[120:121], v131 offset:24576\n\t" \
        "v_mfma_f32_16x16x32_bf16 v[24:27], v[104:107], v[68:71], v[24:27]\n\t" \
        "ds_read_b64_tr_b16 v[122:123], v131 offset:26624\n\t" \
        "v_mfma_f32_16x16x32_bf16 v[40:43], v[104:107], v[72:75], v[40:43]\n\t" \
        "ds_read_b64_tr_b16 v[124:125], v132 offset:24576\n\t" \
        "s_add_u32 s43, s43, 1\n\t" \
        "v_mfma_f32_16x16x32_bf16 v[56:59], v[104:107], v[76:79], v[56:59]\n\t" \
        "s_cmp_eq_u32 s43, 4\n\t" \
        "v_mfma_f32_16x16x32_bf16 v[12:15], v[108:111], v[64:67], v[12:15]\n\t" \
        "ds_read_b64_tr_b16 v[126:127], v132 offset:26624\n\t" \
        "s_cselect_b32 s44, s46, s45\n\t" \
        "v_mfma_f32_16x16x32_bf16 v[28:31], v[108:111], v[68:71], v[28:31]\n\t" \
        "s_cselect_b32 s43, 0, s43\n\t" \
        "v_mfma_f32_16x16x32_bf16 v[44:47], v[108:111], v[72:75], v[44:47]\n\t" \
        "v_mfma_f32_16x16x32_bf16 v[60:63], v[108:111], v[76:79], v[60:63]\n\t" \
        "s_waitcnt lgkmcnt(0)\n\t" \
        "s_barrier\n\t" \
        "v_mfma_f32_16x16x32_bf16 v[0:3], v[112:115], v[80:83], v[0:3]\n\t" \
        "v_add_u32_e32 v128, s44, v128\n\t" \
        "v_add_u32_e32 v129, s44, v129\n\t" \
        "v_mfma_f32_16x16x32_bf16 v[16:19], v[112:115], v[84:87], v[16:19]\n\t" \
        "v_add_u32_e32 v130, s44, v130\n\t" \
        "v_mfma_f32_16x16x32_bf16 v[32:35], v[112:115], v[88:91], v[32:35]\n\t" \
        "v_add_u32_e32 v131, s44, v131\n\t" \
        "v_mfma_f32_16x16x32_bf16 v[48:51], v[112:115], v[92:95], v[48:51]\n\t" \
        "v_add_u32_e32 v132, s44, v132\n\t" \
        "ds_read_b64_tr_b16 v[96:97], v129 offset:16384\n\t" \
        "v_mfma_f32_16x16x32_bf16 v[4:7], v[116:119], v[80:83], v[4:7]\n\t" \
        "ds_read_b64_tr_b16 v[98:99], v129 offset:18432\n\t" \
        "v_mfma_f32_16x16x32_bf16 v[20:23], v[116:119], v[84:87], v[20:23]\n\t" \
        "ds_read_b128 v[64:67], v128 offset:0\n\t" \
        "v_mfma_f32_16x16x32_bf16 v[36:39], v[116:119], v[88:91], v[36:39]\n\t" \
        "ds_read_b128 v[68:71], v128 offset:2048\n\t" \
        "ds_read_b128 v[72:75], v128 offset:4096\n\t" \
        "v_mfma_f32_16x16x32_bf16 v[52:55], v[116:119], v[92:95], v[52:55]\n\t" \
        "ds_read_b128 v[76:79], v128 offset:6144\n\t" \
        "v_mfma_f32_16x16x32_bf16 v[8:11], v[120:123], v[80:83], v[8:11]\n\t" \
        "ds_read_b64_tr_b16 v[100:101], v130 offset:16384\n\t" \
        "v_mfma_f32_16x16x32_bf16 v[24:27], v[120:123], v[84:87], v[24:27]\n\t" \
        "ds_read_b64_tr_b16 v[102:103], v130 offset:18432\n\t" \
        "ds_read_b64_tr_b16 v[104:105], v131 offset:16384\n\t" \
        "v_mfma_f32_16x16x32_bf16 v[40:43], v[120:123], v[88:91], v[40:43]\n\t" \
        "ds_read_b64_tr_b16 v[106:107], v131 offset:18432\n\t" \
        "v_mfma_f32_16x16x32_bf16 v[56:59], v[120:123], v[92:95], v[56:59]\n\t" \
        "ds_read_b64_tr_b16 v[108:109], v132 offset:16384\n\t" \
        "v_mfma_f32_16x16x32_bf16 v[12:15], v[124:127], v[80:83], v[12:15]\n\t" \
        "ds_read_b64_tr_b16 v[110:111], v132 offset:18432\n\t" \
        "v_mfma_f32_16x16x32_bf16 v[28:31], v[124:127], v[84:87], v[28:31]\n\t" \
        "v_mfma_f32_16x16x32_bf16 v[44:47], v[124:127], v[88:91], v[44:47]\n\t" \
        "v_mfma_f32_16x16x32_bf16 v[60:63], v[124:127], v[92:95], v[60:63]\n\t" \
        "s_waitcnt lgkmcnt(0)\n\t" \
        "v_mfma_f32_16x16x32_bf16 v[0:3], v[96:99], v[64:67], v[0:3]\n\t" \
        "ds_read_b64_tr_b16 v[112:113], v129 offset:24576\n\t" \
        "v_mfma_f32_16x16x32_bf16 v[16:19], v[96:99], v[68:71], v[16:19]\n\t" \
        "ds_read_b64_tr_b16 v[114:115], v129 offset:26624\n\t" \
        "v_mfma_f32_16x16x32_bf16 v[32:35], v[96:99], v[72:75], v[32:35]\n\t" \
        "ds_read_b128 v[80:83], v128 offset:1024\n\t" \
        "v_mfma_f32_16x16x32_bf16 v[48:51], v[96:99], v[76:79], v[48:51]\n\t" \
        "ds_read_b128 v[84:87], v128 offset:3072\n\t" \
        "v_mfma_f32_16x16x32_bf16 v[4:7], v[100:103], v[64:67], v[4:7]\n\t" \
        "ds_read_b128 v[88:91], v128 offset:5120\n\t" \
        "v_mfma_f32_16x16x32_bf16 v[20:23], v[100:103], v[68:71], v[20:23]\n\t" \
        "ds_read_b128 v[92:95], v128 offset:7168\n\t" \
        "v_mfma_f32_16x16x32_bf16 v[36:39], v[100:103], v[72:75], v[36:39]\n\t" \
        "ds_read_b64_tr_b16 v[116:117], v130 offset:24576\n\t" \
        "v_mfma_f32_16x16x32_bf16 v[52:55], v[100:103], v[76:79], v[52:55]\n\t" \
        "ds_read_b64_tr_b16 v[118:119], v130 offset:26624\n\t" \
        "v_mfma_f32_16x16x32_bf16 v[8:11], v[104:107], v[64:67], v[8:11]\n\t" \
        "ds_read_b64_tr_b16 v[120:121], v131 offset:24576\n\t" \
        "v_mfma_f32_16x16x32_bf16 v[24:27], v[104:107], v[68:71], v[24:27]\n\t" \
        "ds_read_b64_tr_b16 v[122:123], v131 offset:26624\n\t" \
        "v_mfma_f32_16x16x32_bf16 v[40:43], v[104:107], v[72:75], v[40:43]\n\t" \
        "ds_read_b64_tr_b16 v[124:125], v132 offset:24576\n\t" \
        "s_add_u32 s43, s43, 1\n\t" \
        "v_mfma_f32_16x16x32_bf16 v[56:59], v[104:107], v[76:79], v[56:59]\n\t" \
        "s_cmp_eq_u32 s43, 4\n\t" \
        "v_mfma_f32_16x16x32_bf16 v[12:15], v[108:111], v[64:67], v[12:15]\n\t" \
        "ds_read_b64_tr_b16 v[126:127], v132 offset:26624\n\t" \
        "s_cselect_b32 s44, s46, s45\n\t" \
        "v_mfma_f32_16x16x32_bf16 v[28:31], v[108:111], v[68:71], v[28:31]\n\t" \
        "s_cselect_b32 s43, 0, s43\n\t" \
        "v_mfma_f32_16x16x32_bf16 v[44:47], v[108:111], v[72:75], v[44:47]\n\t" \
        "v_mfma_f32_16x16x32_bf16 v[60:63], v[108:111], v[76:79], v[60:63]\n\t" \
        "s_waitcnt lgkmcnt(0)\n\t" \
        "s_barrier\n\t" \
        "v_mfma_f32_16x16x32_bf16 v[0:3], v[112:115], v[80:83], v[0:3]\n\t" \
        "v_add_u32_e32 v128, s44, v128\n\t" \
        "v_add_u32_e32 v129, s44, v129\n\t" \
        "v_mfma_f32_16x16x32_bf16 v[16:19], v[112:115], v[84:87], v[16:19]\n\t" \
        "v_add_u32_e32 v130, s44, v130\n\t" \
        "v_mfma_f32_16x16x32_bf16 v[32:35], v[112:115], v[88:91], v[32:35]\n\t" \
        "v_add_u32_e32 v131, s44, v131\n\t" \
        "v_mfma_f32_16x16x32_bf16 v[48:51], v[112:115], v[92:95], v[48:51]\n\t" \
        "v_add_u32_e32 v132, s44, v132\n\t" \
        "ds_read_b64_tr_b16 v[96:97], v129 offset:16384\n\t" \
        "v_mfma_f32_16x16x32_bf16 v[4:7], v[116:119], v[80:83], v[4:7]\n\t" \
        "ds_read_b64_tr_b16 v[98:99], v129 offset:18432\n\t" \
        "v_mfma_f32_16x16x32_bf16 v[20:23], v[116:119], v[84:87], v[20:23]\n\t" \
        "ds_read_b128 v[64:67], v128 offset:0\n\t" \
        "v_mfma_f32_16x16x32_bf16 v[36:39], v[116:119], v[88:91], v[36:39]\n\t" \
        "ds_read_b128 v[68:71], v128 offset:2048\n\t" \
        "ds_read_b128 v[72:75], v128 offset:4096\n\t" \
        "v_mfma_f32_16x16x32_bf16 v[52:55], v[116:119], v[92:95], v[52:55]\n\t" \
        "ds_read_b128 v[76:79], v128 offset:6144\n\t" \
        "v_mfma_f32_16x16x32_bf16 v[8:11], v[120:123], v[80:83], v[8:11]\n\t" \
        "ds_read_b64_tr_b16 v[100:101], v130 offset:16384\n\t" \
        "v_mfma_f32_16x16x32_bf16 v[24:27], v[120:123], v[84:87], v[24:27]\n\t" \
        "ds_read_b64_tr_b16 v[102:103], v130 offset:18432\n\t" \
        "ds_read_b64_tr_b16 v[104:105], v131 offset:16384\n\t" \
        "v_mfma_f32_16x16x32_bf16 v[40:43], v[120:123], v[88:91], v[40:43]\n\t" \
        "ds_read_b64_tr_b16 v[106:107], v131 offset:18432\n\t" \
        "v_mfma_f32_16x16x32_bf16 v[56:59], v[120:123], v[92:95], v[56:59]\n\t" \
        "ds_read_b64_tr_b16 v[108:109], v132 offset:16384\n\t" \
        "v_mfma_f32_16x16x32_bf16 v[12:15], v[124:127], v[80:83], v[12:15]\n\t" \
        "ds_read_b64_tr_b16 v[110:111], v132 offset:18432\n\t" \
        "v_mfma_f32_16x16x32_bf16 v[28:31], v[124:127], v[84:87], v[28:31]\n\t" \
        "v_mfma_f32_16x16x32_bf16 v[44:47], v[124:127], v[88:91], v[44:47]\n\t" \
        "v_mfma_f32_16x16x32_bf16 v[60:63], v[124:127], v[92:95], v[60:63]\n\t" \
        "s_waitcnt lgkmcnt(0)\n\t" \
        "v_mfma_f32_16x16x32_bf16 v[0:3], v[96:99], v[64:67], v[0:3]\n\t" \
        "ds_read_b64_tr_b16 v[112:113], v129 offset:24576\n\t" \
        "v_mfma_f32_16x16x32_bf16 v[16:19], v[96:99], v[68:71], v[16:19]\n\t" \
        "ds_read_b64_tr_b16 v[114:115], v129 offset:26624\n\t" \
        "v_mfma_f32_16x16x32_bf16 v[32:35], v[96:99], v[72:75], v[32:35]\n\t" \
        "ds_read_b128 v[80:83], v128 offset:1024\n\t" \
        "v_mfma_f32_16x16x32_bf16 v[48:51], v[96:99], v[76:79], v[48:51]\n\t" \
        "ds_read_b128 v[84:87], v128 offset:3072\n\t" \
        "v_mfma_f32_16x16x32_bf16 v[4:7], v[100:103], v[64:67], v[4:7]\n\t" \
        "ds_read_b128 v[88:91], v128 offset:5120\n\t" \
        "v_mfma_f32_16x16x32_bf16 v[20:23], v[100:103], v[68:71], v[20:23]\n\t" \
        "ds_read_b128 v[92:95], v128 offset:7168\n\t" \
        "v_mfma_f32_16x16x32_bf16 v[36:39], v[100:103], v[72:75], v[36:39]\n\t" \
        "ds_read_b64_tr_b16 v[116:117], v130 offset:24576\n\t" \
        "v_mfma_f32_16x16x32_bf16 v[52:55], v[100:103], v[76:79], v[52:55]\n\t" \
        "ds_read_b64_tr_b16 v[118:119], v130 offset:26624\n\t" \
        "v_mfma_f32_16x16x32_bf16 v[8:11], v[104:107], v[64:67], v[8:11]\n\t" \
        "ds_read_b64_tr_b16 v[120:121], v131 offset:24576\n\t" \
        "v_mfma_f32_16x16x32_bf16 v[24:27], v[104:107], v[68:71], v[24:27]\n\t" \
        "ds_read_b64_tr_b16 v[122:123], v131 offset:26624\n\t" \
        "v_mfma_f32_16x16x32_bf16 v[40:43], v[104:107], v[72:75], v[40:43]\n\t" \
        "ds_read_b64_tr_b16 v[124:125], v132 offset:24576\n\t" \
        "s_add_u32 s43, s43, 1\n\t" \
        "v_mfma_f32_16x16x32_bf16 v[56:59], v[104:107], v[76:79], v[56:59]\n\t" \
        "s_cmp_eq_u32 s43, 4\n\t" \
        "v_mfma_f32_16x16x32_bf16 v[12:15], v[108:111], v[64:67], v[12:15]\n\t" \
        "ds_read_b64_tr_b16 v[126:127], v132 offset:26624\n\t" \
        "s_cselect_b32 s44, s46, s45\n\t" \
        "v_mfma_f32_16x16x32_bf16 v[28:31], v[108:111], v[68:71], v[28:31]\n\t" \
        "s_cselect_b32 s43, 0, s43\n\t" \
        "v_mfma_f32_16x16x32_bf16 v[44:47], v[108:111], v[72:75], v[44:47]\n\t" \
        "v_mfma_f32_16x16x32_bf16 v[60:63], v[108:111], v[76:79], v[60:63]\n\t" \
        "s_waitcnt lgkmcnt(0)\n\t" \
        "s_barrier\n\t" \
        "v_mfma_f32_16x16x32_bf16 v[0:3], v[112:115], v[80:83], v[0:3]\n\t" \
        "v_add_u32_e32 v128, s44, v128\n\t" \
        "v_add_u32_e32 v129, s44, v129\n\t" \
        "v_mfma_f32_16x16x32_bf16 v[16:19], v[112:115], v[84:87], v[16:19]\n\t" \
        "v_add_u32_e32 v130, s44, v130\n\t" \
        "v_mfma_f32_16x16x32_bf16 v[32:35], v[112:115], v[88:91], v[32:35]\n\t" \
        "v_add_u32_e32 v131, s44, v131\n\t" \
        "v_mfma_f32_16x16x32_bf16 v[48:51], v[112:115], v[92:95], v[48:51]\n\t" \
        "v_add_u32_e32 v132, s44, v132\n\t" \
        "ds_read_b64_tr_b16 v[96:97], v129 offset:16384\n\t" \
        "v_mfma_f32_16x16x32_bf16 v[4:7], v[116:119], v[80:83], v[4:7]\n\t" \
        "ds_read_b64_tr_b16 v[98:99], v129 offset:18432\n\t" \
        "v_mfma_f32_16x16x32_bf16 v[20:23], v[116:119], v[84:87], v[20:23]\n\t" \
        "ds_read_b128 v[64:67], v128 offset:0\n\t" \
        "v_mfma_f32_16x16x32_bf16 v[36:39], v[116:119], v[88:91], v[36:39]\n\t" \
        "ds_read_b128 v[68:71], v128 offset:2048\n\t" \
        "ds_read_b128 v[72:75], v128 offset:4096\n\t" \
        "v_mfma_f32_16x16x32_bf16 v[52:55], v[116:119], v[92:95], v[52:55]\n\t" \
        "ds_read_b128 v[76:79], v128 offset:6144\n\t" \
        "v_mfma_f32_16x16x32_bf16 v[8:11], v[120:123], v[80:83], v[8:11]\n\t" \
        "ds_read_b64_tr_b16 v[100:101], v130 offset:16384\n\t" \
        "v_mfma_f32_16x16x32_bf16 v[24:27], v[120:123], v[84:87], v[24:27]\n\t" \
        "ds_read_b64_tr_b16 v[102:103], v130 offset:18432\n\t" \
        "ds_read_b64_tr_b16 v[104:105], v131 offset:16384\n\t" \
        "v_mfma_f32_16x16x32_bf16 v[40:43], v[120:123], v[88:91], v[40:43]\n\t" \
        "ds_read_b64_tr_b16 v[106:107], v131 offset:18432\n\t" \
        "v_mfma_f32_16x16x32_bf16 v[56:59], v[120:123], v[92:95], v[56:59]\n\t" \
        "ds_read_b64_tr_b16 v[108:109], v132 offset:16384\n\t" \
        "v_mfma_f32_16x16x32_bf16 v[12:15], v[124:127], v[80:83], v[12:15]\n\t" \
        "ds_read_b64_tr_b16 v[110:111], v132 offset:18432\n\t" \
        "v_mfma_f32_16x16x32_bf16 v[28:31], v[124:127], v[84:87], v[28:31]\n\t" \
        "v_mfma_f32_16x16x32_bf16 v[44:47], v[124:127], v[88:91], v[44:47]\n\t" \
        "v_mfma_f32_16x16x32_bf16 v[60:63], v[124:127], v[92:95], v[60:63]\n\t" \
        "s_waitcnt lgkmcnt(0)\n\t" \
        "v_mfma_f32_16x16x32_bf16 v[0:3], v[96:99], v[64:67], v[0:3]\n\t" \
        "ds_read_b64_tr_b16 v[112:113], v129 offset:24576\n\t" \
        "v_mfma_f32_16x16x32_bf16 v[16:19], v[96:99], v[68:71], v[16:19]\n\t" \
        "ds_read_b64_tr_b16 v[114:115], v129 offset:26624\n\t" \
        "v_mfma_f32_16x16x32_bf16 v[32:35], v[96:99], v[72:75], v[32:35]\n\t" \
        "ds_read_b128 v[80:83], v128 offset:1024\n\t" \
        "v_mfma_f32_16x16x32_bf16 v[48:51], v[96:99], v[76:79], v[48:51]\n\t" \
        "ds_read_b128 v[84:87], v128 offset:3072\n\t" \
        "v_mfma_f32_16x16x32_bf16 v[4:7], v[100:103], v[64:67], v[4:7]\n\t" \
        "ds_read_b128 v[88:91], v128 offset:5120\n\t" \
        "v_mfma_f32_16x16x32_bf16 v[20:23], v[100:103], v[68:71], v[20:23]\n\t" \
        "ds_read_b128 v[92:95], v128 offset:7168\n\t" \
        "v_mfma_f32_16x16x32_bf16 v[36:39], v[100:103], v[72:75], v[36:39]\n\t" \
        "ds_read_b64_tr_b16 v[116:117], v130 offset:24576\n\t" \
        "v_mfma_f32_16x16x32_bf16 v[52:55], v[100:103], v[76:79], v[52:55]\n\t" \
        "ds_read_b64_tr_b16 v[118:119], v130 offset:26624\n\t" \
        "v_mfma_f32_16x16x32_bf16 v[8:11], v[104:107], v[64:67], v[8:11]\n\t" \
        "ds_read_b64_tr_b16 v[120:121], v131 offset:24576\n\t" \
        "v_mfma_f32_16x16x32_bf16 v[24:27], v[104:107], v[68:71], v[24:27]\n\t" \
        "ds_read_b64_tr_b16 v[122:123], v131 offset:26624\n\t" \
        "v_mfma_f32_16x16x32_bf16 v[40:43], v[104:107], v[72:75], v[40:43]\n\t" \
        "ds_read_b64_tr_b16 v[124:125], v132 offset:24576\n\t" \
        "s_add_u32 s43, s43, 1\n\t" \
        "v_mfma_f32_16x16x32_bf16 v[56:59], v[104:107], v[76:79], v[56:59]\n\t" \
        "s_cmp_eq_u32 s43, 4\n\t" \
        "v_mfma_f32_16x16x32_bf16 v[12:15], v[108:111], v[64:67], v[12:15]\n\t" \
        "ds_read_b64_tr_b16 v[126:127], v132 offset:26624\n\t" \
        "s_cselect_b32 s44, s46, s45\n\t" \
        "v_mfma_f32_16x16x32_bf16 v[28:31], v[108:111], v[68:71], v[28:31]\n\t" \
        "s_cselect_b32 s43, 0, s43\n\t" \
        "v_mfma_f32_16x16x32_bf16 v[44:47], v[108:111], v[72:75], v[44:47]\n\t" \
        "v_mfma_f32_16x16x32_bf16 v[60:63], v[108:111], v[76:79], v[60:63]\n\t" \
        "s_waitcnt lgkmcnt(0)\n\t" \
        "s_barrier\n\t" \
        "v_mfma_f32_16x16x32_bf16 v[0:3], v[112:115], v[80:83], v[0:3]\n\t" \
        "v_add_u32_e32 v128, s44, v128\n\t" \
        "v_add_u32_e32 v129, s44, v129\n\t" \
        "v_mfma_f32_16x16x32_bf16 v[16:19], v[112:115], v[84:87], v[16:19]\n\t" \
        "v_add_u32_e32 v130, s44, v130\n\t" \
        "v_mfma_f32_16x16x32_bf16 v[32:35], v[112:115], v[88:91], v[32:35]\n\t" \
        "v_add_u32_e32 v131, s44, v131\n\t" \
        "v_mfma_f32_16x16x32_bf16 v[48:51], v[112:115], v[92:95], v[48:51]\n\t" \
        "v_add_u32_e32 v132, s44, v132\n\t" \
        "ds_read_b64_tr_b16 v[96:97], v129 offset:16384\n\t" \
        "v_mfma_f32_16x16x32_bf16 v[4:7], v[116:119], v[80:83], v[4:7]\n\t" \
        "ds_read_b64_tr_b16 v[98:99], v129 offset:18432\n\t" \
        "v_mfma_f32_16x16x32_bf16 v[20:23], v[116:119], v[84:87], v[20:23]\n\t" \
        "ds_read_b128 v[64:67], v128 offset:0\n\t" \
        "v_mfma_f32_16x16x32_bf16 v[36:39], v[116:119], v[88:91], v[36:39]\n\t" \
        "ds_read_b128 v[68:71], v128 offset:2048\n\t" \
        "ds_read_b128 v[72:75], v128 offset:4096\n\t" \
        "v_mfma_f32_16x16x32_bf16 v[52:55], v[116:119], v[92:95], v[52:55]\n\t" \
        "ds_read_b128 v[76:79], v128 offset:6144\n\t" \
        "v_mfma_f32_16x16x32_bf16 v[8:11], v[120:123], v[80:83], v[8:11]\n\t" \
        "ds_read_b64_tr_b16 v[100:101], v130 offset:16384\n\t" \
        "v_mfma_f32_16x16x32_bf16 v[24:27], v[120:123], v[84:87], v[24:27]\n\t" \
        "ds_read_b64_tr_b16 v[102:103], v130 offset:18432\n\t" \
        "ds_read_b64_tr_b16 v[104:105], v131 offset:16384\n\t" \
        "v_mfma_f32_16x16x32_bf16 v[40:43], v[120:123], v[88:91], v[40:43]\n\t" \
        "ds_read_b64_tr_b16 v[106:107], v131 offset:18432\n\t" \
        "v_mfma_f32_16x16x32_bf16 v[56:59], v[120:123], v[92:95], v[56:59]\n\t" \
        "ds_read_b64_tr_b16 v[108:109], v132 offset:16384\n\t" \
        "v_mfma_f32_16x16x32_bf16 v[12:15], v[124:127], v[80:83], v[12:15]\n\t" \
        "ds_read_b64_tr_b16 v[110:111], v132 offset:18432\n\t" \
        "v_mfma_f32_16x16x32_bf16 v[28:31], v[124:127], v[84:87], v[28:31]\n\t" \
        "v_mfma_f32_16x16x32_bf16 v[44:47], v[124:127], v[88:91], v[44:47]\n\t" \
        "v_mfma_f32_16x16x32_bf16 v[60:63], v[124:127], v[92:95], v[60:63]\n\t" \
        "s_add_u32 s42, s42, 4\n\t" \
        "s_cmp_lt_u32 s42, 16\n\t" \
        "s_cbranch_scc1 1b\n\t" \
        "s_waitcnt lgkmcnt(0)\n\t" \
        "s_nop 7\n\t" \
        "s_nop 1\n\t"
#define OPROJ_CLOBBERS "v64", "v65", "v66", "v67", "v68", "v69", "v70", "v71", "v72", "v73", "v74", "v75", "v76", "v77", "v78", "v79", "v80", "v81", "v82", "v83", "v84", "v85", "v86", "v87", "v88", "v89", "v90", "v91", "v92", "v93", "v94", "v95", "v96", "v97", "v98", "v99", "v100", "v101", "v102", "v103", "v104", "v105", "v106", "v107", "v108", "v109", "v110", "v111", "v112", "v113", "v114", "v115", "v116", "v117", "v118", "v119", "v120", "v121", "v122", "v123", "v124", "v125", "v126", "v127", "s42", "s43", "s44", "s45", "s46"

constexpr int P1_X = 0, P1_W = 16384, P1_S = 49152, P1_SLOT = 51200, P1_DUMMY = 3 * P1_SLOT, P1_LDS = P1_DUMMY + 256;

DI void proj_loader(char* smem, int lw, int lane, const char* xg, const char* wg, int ldw  , const char* sg  , int wgi  ,
                    const char* tx, const char* tw) {
    const unsigned lds0 = (unsigned)(uintptr_t)smem;
    const unsigned xo = (unsigned)((lane >> 3) * 2048 + (((lane & 7) ^ (4 * (lw & 1) + (lane >> 4))) << 4));
    unsigned wo[2];
#pragma unroll
    for (int e = 0; e < 2; ++e) { const int f = ((2 * (lw & 1) + (lane >> 5)) & 3) | (e << 2); wo[e] = (unsigned)((lane >> 5) * ldw + (((lane & 31) ^ (f << 1)) << 4)); }
    const unsigned so = (unsigned)((lane >> 1) * 64 + (lane & 1) * 16);
    const bool strip = sg != nullptr && lw < 2;
    const char* xs = xg + lw * 8 * 2048; const char* wsrc = wg + (size_t)(2 * lw) * ldw; const char* ss = sg + lw * 2048;
    const size_t wstep = (size_t)8 * ldw;
    auto stage = [&](int slot) {
        const unsigned l = lds0 + slot * P1_SLOT + lw * 1024;
#pragma unroll
        for (int j = 0; j < 4; ++j) glds16<0>(xo, xs + j * 32 * 2048, l + P1_X + j * 4096);
#pragma unroll
        for (int j = 0; j < 8; ++j) glds16<0>(wo[j & 1], wsrc + j * wstep, l + P1_W + j * 4096);
        if (strip) glds16<0>(so, ss, l + P1_S);
        xs += 128; wsrc += 8 * wstep; ss += 4096;
    };
    if (lw == 3) touch64(lane < 16 ? tx + (size_t)lane * 4096 : tw + (size_t)((lane - 16) & 31) * ldw, lds0 + P1_DUMMY);
    stage(0);
    asm volatile("s_waitcnt vmcnt(0)\n\ts_barrier" ::: "memory");
    int fill = 1;
#pragma unroll 1
    for (int t = -1; t < 14; ++t) {
        stage(fill);
        if (t >= 0) { if (strip) WAIT_BAR(13); else WAIT_BAR(12); }
        fill = fill == 2 ? 0 : fill + 1;
    }
    WAIT_BAR(0); WAIT_BAR(0);
    if (lw == 2 && wgi < 7) { const char* code; CODE_ADDR(code, mqa_mha_attn_kernel); touch64(code + (wgi * 64 + lane) * 64, lds0 + P1_DUMMY); WAIT_VM0(); }
}

template <int MODE>
DI void proj_mfma(char* smem, int w, int lane, char* dst  , float scale, int hstr, int tstr  ,
                  char* kvA  , int ct) {
    const int c16 = lane & 15, g = lane >> 4, q4 = c16 >> 2, p4 = lane & 3;
    const unsigned lds0 = (unsigned)(uintptr_t)smem;
    {
        const char* pc; asm volatile("s_getpc_b64 %0" : "=s"(pc));
        touch64(pc + (w * 64 + lane) * 64, lds0 + P1_DUMMY);
    }
    f32x16 acc[8]; f32x4 sacc[2];
    {
        unsigned wa[4];
#pragma unroll
        for (int j = 0; j < 4; ++j) wa[j] = lds0 + (8 * g + q4) * 512 + (((4 * w + j) ^ (q4 | ((g & 1) << 2))) << 5) + p4 * 8;
        const unsigned xa0 = lds0 + P1_X + c16 * 128 + ((g ^ ((c16 >> 1) & 7)) << 4);
#define PROJ_ACC "+{v[0:15]}"(acc[0]), "+{v[16:31]}"(acc[1]), "+{v[32:47]}"(acc[2]), "+{v[48:63]}"(acc[3]), "+{v[64:79]}"(acc[4]), "+{v[80:95]}"(acc[5]), "+{v[96:111]}"(acc[6]), "+{v[112:127]}"(acc[7])
#pragma unroll
        for (int i = 0; i < 8; ++i) acc[i] = f32x16{};
        sacc[0] = f32x4{}; sacc[1] = f32x4{};
        asm volatile("" : PROJ_ACC);
        if (MODE == 2) {
            unsigned xa[4];
#pragma unroll
            for (int j = 0; j < 4; ++j) xa[j] = xa0 + ((w + j) & 3) * 4096;
            unsigned swa = lds0 + (8 * g + q4) * 32 + p4 * 8;
            asm volatile("s_barrier" ::: "memory");
            asm volatile(PROJ_QS_ASM
                : PROJ_ACC, "+{v[232:235]}"(sacc[0]), "+{v[236:239]}"(sacc[1]),
                  "+{v224}"(xa[0]), "+{v225}"(xa[1]), "+{v226}"(xa[2]), "+{v227}"(xa[3]),
                  "+{v228}"(wa[0]), "+{v229}"(wa[1]), "+{v230}"(wa[2]), "+{v231}"(wa[3]), "+{v244}"(swa) :
                : PROJ_QS_CLOBBERS, "scc", "memory");
        } else {
            unsigned x0 = xa0;
            asm volatile("s_barrier" ::: "memory");
            if (MODE == 1) asm volatile(PROJ_V_ASM : PROJ_ACC, "+{v224}"(x0), "+{v225}"(wa[0]), "+{v226}"(wa[1]), "+{v227}"(wa[2]), "+{v228}"(wa[3]) : : PROJ_V_CLOBBERS, "scc", "memory");
            else           asm volatile(PROJ_QK_ASM : PROJ_ACC, "+{v224}"(x0), "+{v225}"(wa[0]), "+{v226}"(wa[1]), "+{v227}"(wa[2]), "+{v228}"(wa[3]) : : PROJ_QK_CLOBBERS, "scc", "memory");
        }
    }
    WAIT_VM0();
    if (MODE == 2) {
#pragma unroll
        for (int e = 0; e < 2; ++e) {
            const unsigned lo = cvtpk(sacc[e][0], sacc[e][1]), hi = cvtpk(sacc[e][2], sacc[e][3]);
            char* d = kvA + w * 4096;
            if (ct < 2) {
                *(u32x2*)(d + (g >> 1) * 1024 + (16 * e + c16 + 32 * (g & 1)) * 16 + ct * 8) = (u32x2){lo, hi};
            } else {
                typedef __attribute__((address_space(3))) unsigned short* lds_u16; typedef __attribute__((address_space(3))) u32x2* lds_u2;
                lds_u16 tp = (lds_u16)((lds_ptr)smem + w * 1024 + e * 512 + (4 * g) * 32 + c16 * 2);
                tp[0] = (unsigned short)lo; tp[16] = (unsigned short)(lo >> 16); tp[32] = (unsigned short)hi; tp[48] = (unsigned short)(hi >> 16);
                const u32x2 v = *(lds_u2)((lds_ptr)smem + w * 1024 + e * 512 + c16 * 32 + g * 8);
                *(u32x2*)(d + 2048 + e * 1024 + (16 * (ct & 1) + c16 + 32 * (g & 1)) * 16 + (g >> 1) * 8) = v;
            }
        }
    }
#pragma unroll
    for (int i = 0; i < 8; ++i)
#pragma unroll
        for (int hh = 0; hh < 2; ++hh) {
            f32x4 a = sub4(acc[i], 2 * hh), b = sub4(acc[i], 2 * hh + 1);
            if (MODE != 1) {
                const int tb = MODE == 2 ? ((w + (i >> 1)) & 3) : (i >> 1);
                char* d = dst + (size_t)(2 * w + hh) * hstr + tb * tstr + (g >> 1) * 1024 + (16 * (i & 1) + c16 + 32 * (g & 1)) * 16;
                st16<ST0>(d, pack44(a * scale, b * scale));
            } else {
                const auto r0 = __builtin_amdgcn_permlane32_swap(cvtpk(a[0], a[1]), cvtpk(b[0], b[1]), false, false);
                const auto r1 = __builtin_amdgcn_permlane32_swap(cvtpk(a[2], a[3]), cvtpk(b[2], b[3]), false, false);
                char* d = dst + (size_t)(2 * w + hh) * 262144 + (i >> 1) * 4096 + 2048 + (i & 1) * 1024 + (16 * (g >> 1) + c16 + 32 * (g & 1)) * 16;
                st16<ST0>(d, (u32x4){r0[0], r1[0], r0[1], r1[1]});
            }
        }
}

extern "C" __global__ __launch_bounds__(512) void mqa_mha_proj_kernel(const char* __restrict__ xA, const char* __restrict__ wq, const char* __restrict__ wk, const char* __restrict__ wv,
                                                                      const char* __restrict__ xB, const char* __restrict__ wqkv, char* __restrict__ ws) {
    extern __shared__ __attribute__((aligned(1024))) char smem[];
    const int tid = threadIdx.x, lane = tid & 63, w = __builtin_amdgcn_readfirstlane(tid >> 6);
    const int b = blockIdx.x, xcd = b & 7, idx = b >> 3, cg = xcd & 3, rb = (xcd >> 2) * 8 + (idx & 7), ct = idx >> 3;
    if (__builtin_expect(w >= 4, 1)) {
        const char* x = cg == 0 ? xA : xB; const char* wb = cg == 0 ? wq : wqkv + (cg - 1) * 2048; const int ldw = cg == 0 ? 2048 : 6144;
        const char* sg = cg == 0 ? (ct < 2 ? wk : wv) + (ct & 1) * 32 : nullptr;
        proj_loader(smem, w - 4, lane, x + (size_t)rb * 128 * 2048, wb + ct * 512, ldw, sg, idx,
                    x + (size_t)((xcd >> 2) * 1024 + idx * 32) * 2048, wb + (size_t)(idx * 32) * ldw);
        return;
    }
    char* qd = ws + WS_QF + (size_t)((cg ? 32 : 0) + ct * 8) * 131072 + rb * 4 * 2048;
    char* kd = ws + WS_KV + (size_t)(ct * 8) * 262144 + rb * 4 * 4096;
    if (cg == 0)      proj_mfma<2>(smem, w, lane, qd, QSCALE, 131072, 2048, ws + WS_KV + (size_t)32 * 262144 + rb * 4 * 4096, ct);
    else if (cg == 3) proj_mfma<1>(smem, w, lane, kd, 1.0f, 262144, 4096, nullptr, ct);
    else              proj_mfma<0>(smem, w, lane, cg == 1 ? qd : kd, cg == 1 ? QSCALE : 1.0f, cg == 1 ? 131072 : 262144, cg == 1 ? 2048 : 4096, nullptr, ct);
}

#define ATTN_LOOP_ASM \
        "ds_read_b128 v[144:147], v181 offset:0\n\t" \
        "ds_read_b128 v[148:151], v181 offset:1024\n\t" \
        "ds_read_b128 v[160:163], v181 offset:2048\n\t" \
        "ds_read_b128 v[164:167], v181 offset:3072\n\t" \
        "s_waitcnt lgkmcnt(0)\n\t" \
        "v_mfma_f32_32x32x16_bf16 v[112:127], v[144:147], v[80:83], 0\n\t" \
        "v_mfma_f32_32x32x16_bf16 v[112:127], v[148:151], v[84:87], v[112:127]\n\t" \
        "s_nop 7\n\t" \
        "s_nop 7\n\t" \
        "1:\n\t" \
        "v_mfma_f32_32x32x16_bf16 v[128:143], v[144:147], v[88:91], 0\n\t" \
        "v_exp_f32_e32 v112, v112\n\t" \
        "v_exp_f32_e32 v113, v113\n\t" \
        "v_exp_f32_e32 v114, v114\n\t" \
        "v_cvt_pk_bf16_f32 v184, v112, v113\n\t" \
        "v_mfma_f32_32x32x16_bf16 v[48:63], v[172:175], v[196:199], v[48:63]\n\t" \
        "ds_read_b128 v[152:155], v181 offset:4096\n\t" \
        "v_exp_f32_e32 v115, v115\n\t" \
        "v_exp_f32_e32 v116, v116\n\t" \
        "v_exp_f32_e32 v117, v117\n\t" \
        "v_cvt_pk_bf16_f32 v185, v114, v115\n\t" \
        "v_smfmac_f32_16x16x64_bf16 v[76:79], v[176:179], v[192:199], v180\n\t" \
        "v_exp_f32_e32 v118, v118\n\t" \
        "v_exp_f32_e32 v119, v119\n\t" \
        "v_cvt_pk_bf16_f32 v186, v116, v117\n\t" \
        "v_cvt_pk_bf16_f32 v187, v118, v119\n\t" \
        "v_mfma_f32_32x32x16_bf16 v[128:143], v[148:151], v[92:95], v[128:143]\n\t" \
        "v_exp_f32_e32 v120, v120\n\t" \
        "v_exp_f32_e32 v121, v121\n\t" \
        "v_exp_f32_e32 v122, v122\n\t" \
        "v_cvt_pk_bf16_f32 v188, v120, v121\n\t" \
        "v_mfma_f32_32x32x16_bf16 v[0:15], v[160:163], v[184:187], v[0:15]\n\t" \
        "ds_read_b128 v[156:159], v181 offset:5120\n\t" \
        "v_exp_f32_e32 v123, v123\n\t" \
        "v_exp_f32_e32 v124, v124\n\t" \
        "v_exp_f32_e32 v125, v125\n\t" \
        "v_cvt_pk_bf16_f32 v189, v122, v123\n\t" \
        "v_exp_f32_e32 v126, v126\n\t" \
        "v_exp_f32_e32 v127, v127\n\t" \
        "v_cvt_pk_bf16_f32 v190, v124, v125\n\t" \
        "v_cvt_pk_bf16_f32 v191, v126, v127\n\t" \
        "v_mfma_f32_32x32x16_bf16 v[112:127], v[144:147], v[96:99], 0\n\t" \
        "v_exp_f32_e32 v128, v128\n\t" \
        "v_exp_f32_e32 v129, v129\n\t" \
        "v_exp_f32_e32 v130, v130\n\t" \
        "v_cvt_pk_bf16_f32 v192, v128, v129\n\t" \
        "v_mfma_f32_32x32x16_bf16 v[0:15], v[164:167], v[188:191], v[0:15]\n\t" \
        "ds_read_b128 v[168:171], v181 offset:6144\n\t" \
        "v_exp_f32_e32 v131, v131\n\t" \
        "v_exp_f32_e32 v132, v132\n\t" \
        "v_exp_f32_e32 v133, v133\n\t" \
        "v_cvt_pk_bf16_f32 v193, v130, v131\n\t" \
        "v_smfmac_f32_16x16x64_bf16 v[64:67], v[176:179], v[184:191], v180\n\t" \
        "v_exp_f32_e32 v134, v134\n\t" \
        "v_exp_f32_e32 v135, v135\n\t" \
        "v_cvt_pk_bf16_f32 v194, v132, v133\n\t" \
        "v_cvt_pk_bf16_f32 v195, v134, v135\n\t" \
        "v_mfma_f32_32x32x16_bf16 v[112:127], v[148:151], v[100:103], v[112:127]\n\t" \
        "v_exp_f32_e32 v136, v136\n\t" \
        "v_exp_f32_e32 v137, v137\n\t" \
        "v_exp_f32_e32 v138, v138\n\t" \
        "v_cvt_pk_bf16_f32 v196, v136, v137\n\t" \
        "v_mfma_f32_32x32x16_bf16 v[16:31], v[160:163], v[192:195], v[16:31]\n\t" \
        "ds_read_b128 v[172:175], v181 offset:7168\n\t" \
        "v_exp_f32_e32 v139, v139\n\t" \
        "v_exp_f32_e32 v140, v140\n\t" \
        "v_exp_f32_e32 v141, v141\n\t" \
        "v_cvt_pk_bf16_f32 v197, v138, v139\n\t" \
        "v_exp_f32_e32 v142, v142\n\t" \
        "v_exp_f32_e32 v143, v143\n\t" \
        "v_cvt_pk_bf16_f32 v198, v140, v141\n\t" \
        "v_cvt_pk_bf16_f32 v199, v142, v143\n\t" \
        "v_mfma_f32_32x32x16_bf16 v[128:143], v[144:147], v[104:107], 0\n\t" \
        "v_exp_f32_e32 v112, v112\n\t" \
        "v_exp_f32_e32 v113, v113\n\t" \
        "v_exp_f32_e32 v114, v114\n\t" \
        "v_cvt_pk_bf16_f32 v184, v112, v113\n\t" \
        "v_mfma_f32_32x32x16_bf16 v[16:31], v[164:167], v[196:199], v[16:31]\n\t" \
        "v_exp_f32_e32 v115, v115\n\t" \
        "v_exp_f32_e32 v116, v116\n\t" \
        "v_exp_f32_e32 v117, v117\n\t" \
        "v_cvt_pk_bf16_f32 v185, v114, v115\n\t" \
        "v_smfmac_f32_16x16x64_bf16 v[68:71], v[176:179], v[192:199], v180\n\t" \
        "v_exp_f32_e32 v118, v118\n\t" \
        "v_exp_f32_e32 v119, v119\n\t" \
        "v_cvt_pk_bf16_f32 v186, v116, v117\n\t" \
        "v_cvt_pk_bf16_f32 v187, v118, v119\n\t" \
        "v_mfma_f32_32x32x16_bf16 v[128:143], v[148:151], v[108:111], v[128:143]\n\t" \
        "v_exp_f32_e32 v120, v120\n\t" \
        "v_exp_f32_e32 v121, v121\n\t" \
        "v_exp_f32_e32 v122, v122\n\t" \
        "v_cvt_pk_bf16_f32 v188, v120, v121\n\t" \
        "v_mfma_f32_32x32x16_bf16 v[32:47], v[160:163], v[184:187], v[32:47]\n\t" \
        "v_exp_f32_e32 v123, v123\n\t" \
        "v_exp_f32_e32 v124, v124\n\t" \
        "v_exp_f32_e32 v125, v125\n\t" \
        "v_cvt_pk_bf16_f32 v189, v122, v123\n\t" \
        "v_exp_f32_e32 v126, v126\n\t" \
        "v_exp_f32_e32 v127, v127\n\t" \
        "v_cvt_pk_bf16_f32 v190, v124, v125\n\t" \
        "v_cvt_pk_bf16_f32 v191, v126, v127\n\t" \
        "s_waitcnt lgkmcnt(0)\n\t" \
        "v_mfma_f32_32x32x16_bf16 v[112:127], v[152:155], v[80:83], 0\n\t" \
        "v_exp_f32_e32 v128, v128\n\t" \
        "v_exp_f32_e32 v129, v129\n\t" \
        "v_exp_f32_e32 v130, v130\n\t" \
        "v_cvt_pk_bf16_f32 v192, v128, v129\n\t" \
        "v_mfma_f32_32x32x16_bf16 v[32:47], v[164:167], v[188:191], v[32:47]\n\t" \
        "v_exp_f32_e32 v131, v131\n\t" \
        "v_exp_f32_e32 v132, v132\n\t" \
        "v_exp_f32_e32 v133, v133\n\t" \
        "v_cvt_pk_bf16_f32 v193, v130, v131\n\t" \
        "v_smfmac_f32_16x16x64_bf16 v[72:75], v[176:179], v[184:191], v180\n\t" \
        "v_exp_f32_e32 v134, v134\n\t" \
        "v_exp_f32_e32 v135, v135\n\t" \
        "v_cvt_pk_bf16_f32 v194, v132, v133\n\t" \
        "v_cvt_pk_bf16_f32 v195, v134, v135\n\t" \
        "v_mfma_f32_32x32x16_bf16 v[112:127], v[156:159], v[84:87], v[112:127]\n\t" \
        "v_exp_f32_e32 v136, v136\n\t" \
        "v_exp_f32_e32 v137, v137\n\t" \
        "v_exp_f32_e32 v138, v138\n\t" \
        "v_cvt_pk_bf16_f32 v196, v136, v137\n\t" \
        "v_mfma_f32_32x32x16_bf16 v[48:63], v[160:163], v[192:195], v[48:63]\n\t" \
        "v_exp_f32_e32 v139, v139\n\t" \
        "v_exp_f32_e32 v140, v140\n\t" \
        "v_exp_f32_e32 v141, v141\n\t" \
        "v_cvt_pk_bf16_f32 v197, v138, v139\n\t" \
        "v_exp_f32_e32 v142, v142\n\t" \
        "v_exp_f32_e32 v143, v143\n\t" \
        "v_cvt_pk_bf16_f32 v198, v140, v141\n\t" \
        "v_cvt_pk_bf16_f32 v199, v142, v143\n\t" \
        "v_mfma_f32_32x32x16_bf16 v[128:143], v[152:155], v[88:91], 0\n\t" \
        "v_exp_f32_e32 v112, v112\n\t" \
        "v_exp_f32_e32 v113, v113\n\t" \
        "v_exp_f32_e32 v114, v114\n\t" \
        "v_cvt_pk_bf16_f32 v184, v112, v113\n\t" \
        "v_mfma_f32_32x32x16_bf16 v[48:63], v[164:167], v[196:199], v[48:63]\n\t" \
        "ds_read_b128 v[144:147], v181 offset:8192\n\t" \
        "v_exp_f32_e32 v115, v115\n\t" \
        "v_exp_f32_e32 v116, v116\n\t" \
        "v_exp_f32_e32 v117, v117\n\t" \
        "v_cvt_pk_bf16_f32 v185, v114, v115\n\t" \
        "v_smfmac_f32_16x16x64_bf16 v[76:79], v[176:179], v[192:199], v180\n\t" \
        "v_exp_f32_e32 v118, v118\n\t" \
        "v_exp_f32_e32 v119, v119\n\t" \
        "v_cvt_pk_bf16_f32 v186, v116, v117\n\t" \
        "v_cvt_pk_bf16_f32 v187, v118, v119\n\t" \
        "v_mfma_f32_32x32x16_bf16 v[128:143], v[156:159], v[92:95], v[128:143]\n\t" \
        "v_exp_f32_e32 v120, v120\n\t" \
        "v_exp_f32_e32 v121, v121\n\t" \
        "v_exp_f32_e32 v122, v122\n\t" \
        "v_cvt_pk_bf16_f32 v188, v120, v121\n\t" \
        "v_mfma_f32_32x32x16_bf16 v[0:15], v[168:171], v[184:187], v[0:15]\n\t" \
        "ds_read_b128 v[148:151], v181 offset:9216\n\t" \
        "v_exp_f32_e32 v123, v123\n\t" \
        "v_exp_f32_e32 v124, v124\n\t" \
        "v_exp_f32_e32 v125, v125\n\t" \
        "v_cvt_pk_bf16_f32 v189, v122, v123\n\t" \
        "v_exp_f32_e32 v126, v126\n\t" \
        "v_exp_f32_e32 v127, v127\n\t" \
        "v_cvt_pk_bf16_f32 v190, v124, v125\n\t" \
        "v_cvt_pk_bf16_f32 v191, v126, v127\n\t" \
        "v_mfma_f32_32x32x16_bf16 v[112:127], v[152:155], v[96:99], 0\n\t" \
        "v_exp_f32_e32 v128, v128\n\t" \
        "v_exp_f32_e32 v129, v129\n\t" \
        "v_exp_f32_e32 v130, v130\n\t" \
        "v_cvt_pk_bf16_f32 v192, v128, v129\n\t" \
        "v_mfma_f32_32x32x16_bf16 v[0:15], v[172:175], v[188:191], v[0:15]\n\t" \
        "ds_read_b128 v[160:163], v181 offset:10240\n\t" \
        "v_exp_f32_e32 v131, v131\n\t" \
        "v_exp_f32_e32 v132, v132\n\t" \
        "v_exp_f32_e32 v133, v133\n\t" \
        "v_cvt_pk_bf16_f32 v193, v130, v131\n\t" \
        "v_smfmac_f32_16x16x64_bf16 v[64:67], v[176:179], v[184:191], v180\n\t" \
        "v_exp_f32_e32 v134, v134\n\t" \
        "v_exp_f32_e32 v135, v135\n\t" \
        "v_cvt_pk_bf16_f32 v194, v132, v133\n\t" \
        "v_cvt_pk_bf16_f32 v195, v134, v135\n\t" \
        "v_mfma_f32_32x32x16_bf16 v[112:127], v[156:159], v[100:103], v[112:127]\n\t" \
        "v_exp_f32_e32 v136, v136\n\t" \
        "v_exp_f32_e32 v137, v137\n\t" \
        "v_exp_f32_e32 v138, v138\n\t" \
        "v_cvt_pk_bf16_f32 v196, v136, v137\n\t" \
        "v_mfma_f32_32x32x16_bf16 v[16:31], v[168:171], v[192:195], v[16:31]\n\t" \
        "ds_read_b128 v[164:167], v181 offset:11264\n\t" \
        "v_exp_f32_e32 v139, v139\n\t" \
        "v_exp_f32_e32 v140, v140\n\t" \
        "v_exp_f32_e32 v141, v141\n\t" \
        "v_cvt_pk_bf16_f32 v197, v138, v139\n\t" \
        "v_exp_f32_e32 v142, v142\n\t" \
        "v_exp_f32_e32 v143, v143\n\t" \
        "v_cvt_pk_bf16_f32 v198, v140, v141\n\t" \
        "v_cvt_pk_bf16_f32 v199, v142, v143\n\t" \
        "v_mfma_f32_32x32x16_bf16 v[128:143], v[152:155], v[104:107], 0\n\t" \
        "v_exp_f32_e32 v112, v112\n\t" \
        "v_exp_f32_e32 v113, v113\n\t" \
        "v_exp_f32_e32 v114, v114\n\t" \
        "v_cvt_pk_bf16_f32 v184, v112, v113\n\t" \
        "v_mfma_f32_32x32x16_bf16 v[16:31], v[172:175], v[196:199], v[16:31]\n\t" \
        "v_exp_f32_e32 v115, v115\n\t" \
        "v_exp_f32_e32 v116, v116\n\t" \
        "v_exp_f32_e32 v117, v117\n\t" \
        "v_cvt_pk_bf16_f32 v185, v114, v115\n\t" \
        "v_smfmac_f32_16x16x64_bf16 v[68:71], v[176:179], v[192:199], v180\n\t" \
        "v_exp_f32_e32 v118, v118\n\t" \
        "v_exp_f32_e32 v119, v119\n\t" \
        "v_cvt_pk_bf16_f32 v186, v116, v117\n\t" \
        "v_cvt_pk_bf16_f32 v187, v118, v119\n\t" \
        "v_mfma_f32_32x32x16_bf16 v[128:143], v[156:159], v[108:111], v[128:143]\n\t" \
        "v_exp_f32_e32 v120, v120\n\t" \
        "v_exp_f32_e32 v121, v121\n\t" \
        "v_exp_f32_e32 v122, v122\n\t" \
        "v_cvt_pk_bf16_f32 v188, v120, v121\n\t" \
        "v_mfma_f32_32x32x16_bf16 v[32:47], v[168:171], v[184:187], v[32:47]\n\t" \
        "v_exp_f32_e32 v123, v123\n\t" \
        "v_exp_f32_e32 v124, v124\n\t" \
        "v_exp_f32_e32 v125, v125\n\t" \
        "v_cvt_pk_bf16_f32 v189, v122, v123\n\t" \
        "v_exp_f32_e32 v126, v126\n\t" \
        "v_exp_f32_e32 v127, v127\n\t" \
        "v_cvt_pk_bf16_f32 v190, v124, v125\n\t" \
        "v_cvt_pk_bf16_f32 v191, v126, v127\n\t" \
        "s_waitcnt lgkmcnt(0)\n\t" \
        "v_mfma_f32_32x32x16_bf16 v[112:127], v[144:147], v[80:83], 0\n\t" \
        "v_exp_f32_e32 v128, v128\n\t" \
        "v_exp_f32_e32 v129, v129\n\t" \
        "v_exp_f32_e32 v130, v130\n\t" \
        "v_cvt_pk_bf16_f32 v192, v128, v129\n\t" \
        "v_mfma_f32_32x32x16_bf16 v[32:47], v[172:175], v[188:191], v[32:47]\n\t" \
        "v_exp_f32_e32 v131, v131\n\t" \
        "v_exp_f32_e32 v132, v132\n\t" \
        "v_exp_f32_e32 v133, v133\n\t" \
        "v_cvt_pk_bf16_f32 v193, v130, v131\n\t" \
        "v_smfmac_f32_16x16x64_bf16 v[72:75], v[176:179], v[184:191], v180\n\t" \
        "v_exp_f32_e32 v134, v134\n\t" \
        "v_exp_f32_e32 v135, v135\n\t" \
        "v_cvt_pk_bf16_f32 v194, v132, v133\n\t" \
        "v_cvt_pk_bf16_f32 v195, v134, v135\n\t" \
        "v_mfma_f32_32x32x16_bf16 v[112:127], v[148:151], v[84:87], v[112:127]\n\t" \
        "v_exp_f32_e32 v136, v136\n\t" \
        "v_exp_f32_e32 v137, v137\n\t" \
        "v_exp_f32_e32 v138, v138\n\t" \
        "v_cvt_pk_bf16_f32 v196, v136, v137\n\t" \
        "v_mfma_f32_32x32x16_bf16 v[48:63], v[168:171], v[192:195], v[48:63]\n\t" \
        "v_exp_f32_e32 v139, v139\n\t" \
        "v_exp_f32_e32 v140, v140\n\t" \
        "v_exp_f32_e32 v141, v141\n\t" \
        "v_cvt_pk_bf16_f32 v197, v138, v139\n\t" \
        "v_exp_f32_e32 v142, v142\n\t" \
        "v_exp_f32_e32 v143, v143\n\t" \
        "v_cvt_pk_bf16_f32 v198, v140, v141\n\t" \
        "v_cvt_pk_bf16_f32 v199, v142, v143\n\t" \
        "v_mfma_f32_32x32x16_bf16 v[128:143], v[144:147], v[88:91], 0\n\t" \
        "v_exp_f32_e32 v112, v112\n\t" \
        "v_exp_f32_e32 v113, v113\n\t" \
        "v_exp_f32_e32 v114, v114\n\t" \
        "v_cvt_pk_bf16_f32 v184, v112, v113\n\t" \
        "v_mfma_f32_32x32x16_bf16 v[48:63], v[172:175], v[196:199], v[48:63]\n\t" \
        "ds_read_b128 v[152:155], v181 offset:12288\n\t" \
        "v_exp_f32_e32 v115, v115\n\t" \
        "v_exp_f32_e32 v116, v116\n\t" \
        "v_exp_f32_e32 v117, v117\n\t" \
        "v_cvt_pk_bf16_f32 v185, v114, v115\n\t" \
        "v_smfmac_f32_16x16x64_bf16 v[76:79], v[176:179], v[192:199], v180\n\t" \
        "v_exp_f32_e32 v118, v118\n\t" \
        "v_exp_f32_e32 v119, v119\n\t" \
        "v_cvt_pk_bf16_f32 v186, v116, v117\n\t" \
        "v_cvt_pk_bf16_f32 v187, v118, v119\n\t" \
        "v_mfma_f32_32x32x16_bf16 v[128:143], v[148:151], v[92:95], v[128:143]\n\t" \
        "v_exp_f32_e32 v120, v120\n\t" \
        "v_exp_f32_e32 v121, v121\n\t" \
        "v_exp_f32_e32 v122, v122\n\t" \
        "v_cvt_pk_bf16_f32 v188, v120, v121\n\t" \
        "v_mfma_f32_32x32x16_bf16 v[0:15], v[160:163], v[184:187], v[0:15]\n\t" \
        "ds_read_b128 v[156:159], v181 offset:13312\n\t" \
        "v_exp_f32_e32 v123, v123\n\t" \
        "v_exp_f32_e32 v124, v124\n\t" \
        "v_exp_f32_e32 v125, v125\n\t" \
        "v_cvt_pk_bf16_f32 v189, v122, v123\n\t" \
        "v_exp_f32_e32 v126, v126\n\t" \
        "v_exp_f32_e32 v127, v127\n\t" \
        "v_cvt_pk_bf16_f32 v190, v124, v125\n\t" \
        "v_cvt_pk_bf16_f32 v191, v126, v127\n\t" \
        "v_mfma_f32_32x32x16_bf16 v[112:127], v[144:147], v[96:99], 0\n\t" \
        "v_exp_f32_e32 v128, v128\n\t" \
        "v_exp_f32_e32 v129, v129\n\t" \
        "v_exp_f32_e32 v130, v130\n\t" \
        "v_cvt_pk_bf16_f32 v192, v128, v129\n\t" \
        "v_mfma_f32_32x32x16_bf16 v[0:15], v[164:167], v[188:191], v[0:15]\n\t" \
        "ds_read_b128 v[168:171], v181 offset:14336\n\t" \
        "v_exp_f32_e32 v131, v131\n\t" \
        "v_exp_f32_e32 v132, v132\n\t" \
        "v_exp_f32_e32 v133, v133\n\t" \
        "v_cvt_pk_bf16_f32 v193, v130, v131\n\t" \
        "v_smfmac_f32_16x16x64_bf16 v[64:67], v[176:179], v[184:191], v180\n\t" \
        "v_exp_f32_e32 v134, v134\n\t" \
        "v_exp_f32_e32 v135, v135\n\t" \
        "v_cvt_pk_bf16_f32 v194, v132, v133\n\t" \
        "v_cvt_pk_bf16_f32 v195, v134, v135\n\t" \
        "v_mfma_f32_32x32x16_bf16 v[112:127], v[148:151], v[100:103], v[112:127]\n\t" \
        "v_exp_f32_e32 v136, v136\n\t" \
        "v_exp_f32_e32 v137, v137\n\t" \
        "v_exp_f32_e32 v138, v138\n\t" \
        "v_cvt_pk_bf16_f32 v196, v136, v137\n\t" \
        "v_mfma_f32_32x32x16_bf16 v[16:31], v[160:163], v[192:195], v[16:31]\n\t" \
        "ds_read_b128 v[172:175], v181 offset:15360\n\t" \
        "v_exp_f32_e32 v139, v139\n\t" \
        "v_exp_f32_e32 v140, v140\n\t" \
        "v_exp_f32_e32 v141, v141\n\t" \
        "v_cvt_pk_bf16_f32 v197, v138, v139\n\t" \
        "v_exp_f32_e32 v142, v142\n\t" \
        "v_exp_f32_e32 v143, v143\n\t" \
        "v_cvt_pk_bf16_f32 v198, v140, v141\n\t" \
        "v_cvt_pk_bf16_f32 v199, v142, v143\n\t" \
        "v_mfma_f32_32x32x16_bf16 v[128:143], v[144:147], v[104:107], 0\n\t" \
        "v_exp_f32_e32 v112, v112\n\t" \
        "v_exp_f32_e32 v113, v113\n\t" \
        "v_exp_f32_e32 v114, v114\n\t" \
        "v_cvt_pk_bf16_f32 v184, v112, v113\n\t" \
        "v_mfma_f32_32x32x16_bf16 v[16:31], v[164:167], v[196:199], v[16:31]\n\t" \
        "v_exp_f32_e32 v115, v115\n\t" \
        "v_exp_f32_e32 v116, v116\n\t" \
        "v_exp_f32_e32 v117, v117\n\t" \
        "v_cvt_pk_bf16_f32 v185, v114, v115\n\t" \
        "v_smfmac_f32_16x16x64_bf16 v[68:71], v[176:179], v[192:199], v180\n\t" \
        "v_exp_f32_e32 v118, v118\n\t" \
        "v_exp_f32_e32 v119, v119\n\t" \
        "v_cvt_pk_bf16_f32 v186, v116, v117\n\t" \
        "v_cvt_pk_bf16_f32 v187, v118, v119\n\t" \
        "v_mfma_f32_32x32x16_bf16 v[128:143], v[148:151], v[108:111], v[128:143]\n\t" \
        "v_exp_f32_e32 v120, v120\n\t" \
        "v_exp_f32_e32 v121, v121\n\t" \
        "v_exp_f32_e32 v122, v122\n\t" \
        "v_cvt_pk_bf16_f32 v188, v120, v121\n\t" \
        "v_mfma_f32_32x32x16_bf16 v[32:47], v[160:163], v[184:187], v[32:47]\n\t" \
        "v_exp_f32_e32 v123, v123\n\t" \
        "v_exp_f32_e32 v124, v124\n\t" \
        "v_exp_f32_e32 v125, v125\n\t" \
        "v_cvt_pk_bf16_f32 v189, v122, v123\n\t" \
        "v_exp_f32_e32 v126, v126\n\t" \
        "v_exp_f32_e32 v127, v127\n\t" \
        "v_cvt_pk_bf16_f32 v190, v124, v125\n\t" \
        "v_cvt_pk_bf16_f32 v191, v126, v127\n\t" \
        "s_waitcnt lgkmcnt(0)\n\t" \
        "v_mfma_f32_32x32x16_bf16 v[112:127], v[152:155], v[80:83], 0\n\t" \
        "v_exp_f32_e32 v128, v128\n\t" \
        "v_exp_f32_e32 v129, v129\n\t" \
        "v_exp_f32_e32 v130, v130\n\t" \
        "v_cvt_pk_bf16_f32 v192, v128, v129\n\t" \
        "v_mfma_f32_32x32x16_bf16 v[32:47], v[164:167], v[188:191], v[32:47]\n\t" \
        "v_exp_f32_e32 v131, v131\n\t" \
        "v_exp_f32_e32 v132, v132\n\t" \
        "v_exp_f32_e32 v133, v133\n\t" \
        "v_cvt_pk_bf16_f32 v193, v130, v131\n\t" \
        "v_smfmac_f32_16x16x64_bf16 v[72:75], v[176:179], v[184:191], v180\n\t" \
        "v_exp_f32_e32 v134, v134\n\t" \
        "v_exp_f32_e32 v135, v135\n\t" \
        "v_cvt_pk_bf16_f32 v194, v132, v133\n\t" \
        "v_cvt_pk_bf16_f32 v195, v134, v135\n\t" \
        "v_mfma_f32_32x32x16_bf16 v[112:127], v[156:159], v[84:87], v[112:127]\n\t" \
        "v_exp_f32_e32 v136, v136\n\t" \
        "v_exp_f32_e32 v137, v137\n\t" \
        "v_exp_f32_e32 v138, v138\n\t" \
        "v_cvt_pk_bf16_f32 v196, v136, v137\n\t" \
        "v_mfma_f32_32x32x16_bf16 v[48:63], v[160:163], v[192:195], v[48:63]\n\t" \
        "v_exp_f32_e32 v139, v139\n\t" \
        "v_exp_f32_e32 v140, v140\n\t" \
        "v_exp_f32_e32 v141, v141\n\t" \
        "v_cvt_pk_bf16_f32 v197, v138, v139\n\t" \
        "v_exp_f32_e32 v142, v142\n\t" \
        "v_exp_f32_e32 v143, v143\n\t" \
        "v_cvt_pk_bf16_f32 v198, v140, v141\n\t" \
        "v_cvt_pk_bf16_f32 v199, v142, v143\n\t" \
        "s_barrier\n\t" \
        "v_mfma_f32_32x32x16_bf16 v[128:143], v[152:155], v[88:91], 0\n\t" \
        "v_exp_f32_e32 v112, v112\n\t" \
        "v_exp_f32_e32 v113, v113\n\t" \
        "v_exp_f32_e32 v114, v114\n\t" \
        "v_cvt_pk_bf16_f32 v184, v112, v113\n\t" \
        "v_mfma_f32_32x32x16_bf16 v[48:63], v[164:167], v[196:199], v[48:63]\n\t" \
        "ds_read_b128 v[144:147], v181 offset:16384\n\t" \
        "v_exp_f32_e32 v115, v115\n\t" \
        "v_exp_f32_e32 v116, v116\n\t" \
        "v_exp_f32_e32 v117, v117\n\t" \
        "v_cvt_pk_bf16_f32 v185, v114, v115\n\t" \
        "v_smfmac_f32_16x16x64_bf16 v[76:79], v[176:179], v[192:199], v180\n\t" \
        "v_exp_f32_e32 v118, v118\n\t" \
        "v_exp_f32_e32 v119, v119\n\t" \
        "v_cvt_pk_bf16_f32 v186, v116, v117\n\t" \
        "v_cvt_pk_bf16_f32 v187, v118, v119\n\t" \
        "v_mfma_f32_32x32x16_bf16 v[128:143], v[156:159], v[92:95], v[128:143]\n\t" \
        "v_exp_f32_e32 v120, v120\n\t" \
        "v_exp_f32_e32 v121, v121\n\t" \
        "v_exp_f32_e32 v122, v122\n\t" \
        "v_cvt_pk_bf16_f32 v188, v120, v121\n\t" \
        "v_mfma_f32_32x32x16_bf16 v[0:15], v[168:171], v[184:187], v[0:15]\n\t" \
        "ds_read_b128 v[148:151], v181 offset:17408\n\t" \
        "v_exp_f32_e32 v123, v123\n\t" \
        "v_exp_f32_e32 v124, v124\n\t" \
        "v_exp_f32_e32 v125, v125\n\t" \
        "v_cvt_pk_bf16_f32 v189, v122, v123\n\t" \
        "v_exp_f32_e32 v126, v126\n\t" \
        "v_exp_f32_e32 v127, v127\n\t" \
        "v_cvt_pk_bf16_f32 v190, v124, v125\n\t" \
        "v_cvt_pk_bf16_f32 v191, v126, v127\n\t" \
        "v_mfma_f32_32x32x16_bf16 v[112:127], v[152:155], v[96:99], 0\n\t" \
        "v_exp_f32_e32 v128, v128\n\t" \
        "v_exp_f32_e32 v129, v129\n\t" \
        "v_exp_f32_e32 v130, v130\n\t" \
        "v_cvt_pk_bf16_f32 v192, v128, v129\n\t" \
        "v_mfma_f32_32x32x16_bf16 v[0:15], v[172:175], v[188:191], v[0:15]\n\t" \
        "ds_read_b128 v[160:163], v181 offset:18432\n\t" \
        "v_exp_f32_e32 v131, v131\n\t" \
        "v_exp_f32_e32 v132, v132\n\t" \
        "v_exp_f32_e32 v133, v133\n\t" \
        "v_cvt_pk_bf16_f32 v193, v130, v131\n\t" \
        "v_smfmac_f32_16x16x64_bf16 v[64:67], v[176:179], v[184:191], v180\n\t" \
        "v_exp_f32_e32 v134, v134\n\t" \
        "v_exp_f32_e32 v135, v135\n\t" \
        "v_cvt_pk_bf16_f32 v194, v132, v133\n\t" \
        "v_cvt_pk_bf16_f32 v195, v134, v135\n\t" \
        "v_mfma_f32_32x32x16_bf16 v[112:127], v[156:159], v[100:103], v[112:127]\n\t" \
        "v_exp_f32_e32 v136, v136\n\t" \
        "v_exp_f32_e32 v137, v137\n\t" \
        "v_exp_f32_e32 v138, v138\n\t" \
        "v_cvt_pk_bf16_f32 v196, v136, v137\n\t" \
        "v_mfma_f32_32x32x16_bf16 v[16:31], v[168:171], v[192:195], v[16:31]\n\t" \
        "ds_read_b128 v[164:167], v181 offset:19456\n\t" \
        "v_exp_f32_e32 v139, v139\n\t" \
        "v_exp_f32_e32 v140, v140\n\t" \
        "v_exp_f32_e32 v141, v141\n\t" \
        "v_cvt_pk_bf16_f32 v197, v138, v139\n\t" \
        "v_exp_f32_e32 v142, v142\n\t" \
        "v_exp_f32_e32 v143, v143\n\t" \
        "v_cvt_pk_bf16_f32 v198, v140, v141\n\t" \
        "v_cvt_pk_bf16_f32 v199, v142, v143\n\t" \
        "v_mfma_f32_32x32x16_bf16 v[128:143], v[152:155], v[104:107], 0\n\t" \
        "v_exp_f32_e32 v112, v112\n\t" \
        "v_exp_f32_e32 v113, v113\n\t" \
        "v_exp_f32_e32 v114, v114\n\t" \
        "v_cvt_pk_bf16_f32 v184, v112, v113\n\t" \
        "v_mfma_f32_32x32x16_bf16 v[16:31], v[172:175], v[196:199], v[16:31]\n\t" \
        "v_exp_f32_e32 v115, v115\n\t" \
        "v_exp_f32_e32 v116, v116\n\t" \
        "v_exp_f32_e32 v117, v117\n\t" \
        "v_cvt_pk_bf16_f32 v185, v114, v115\n\t" \
        "v_smfmac_f32_16x16x64_bf16 v[68:71], v[176:179], v[192:199], v180\n\t" \
        "v_exp_f32_e32 v118, v118\n\t" \
        "v_exp_f32_e32 v119, v119\n\t" \
        "v_cvt_pk_bf16_f32 v186, v116, v117\n\t" \
        "v_cvt_pk_bf16_f32 v187, v118, v119\n\t" \
        "v_mfma_f32_32x32x16_bf16 v[128:143], v[156:159], v[108:111], v[128:143]\n\t" \
        "v_exp_f32_e32 v120, v120\n\t" \
        "v_exp_f32_e32 v121, v121\n\t" \
        "v_exp_f32_e32 v122, v122\n\t" \
        "v_cvt_pk_bf16_f32 v188, v120, v121\n\t" \
        "v_mfma_f32_32x32x16_bf16 v[32:47], v[168:171], v[184:187], v[32:47]\n\t" \
        "v_exp_f32_e32 v123, v123\n\t" \
        "v_exp_f32_e32 v124, v124\n\t" \
        "v_exp_f32_e32 v125, v125\n\t" \
        "v_cvt_pk_bf16_f32 v189, v122, v123\n\t" \
        "v_exp_f32_e32 v126, v126\n\t" \
        "v_exp_f32_e32 v127, v127\n\t" \
        "v_cvt_pk_bf16_f32 v190, v124, v125\n\t" \
        "v_cvt_pk_bf16_f32 v191, v126, v127\n\t" \
        "s_waitcnt lgkmcnt(0)\n\t" \
        "v_mfma_f32_32x32x16_bf16 v[112:127], v[144:147], v[80:83], 0\n\t" \
        "v_exp_f32_e32 v128, v128\n\t" \
        "v_exp_f32_e32 v129, v129\n\t" \
        "v_exp_f32_e32 v130, v130\n\t" \
        "v_cvt_pk_bf16_f32 v192, v128, v129\n\t" \
        "v_mfma_f32_32x32x16_bf16 v[32:47], v[172:175], v[188:191], v[32:47]\n\t" \
        "v_exp_f32_e32 v131, v131\n\t" \
        "v_exp_f32_e32 v132, v132\n\t" \
        "v_exp_f32_e32 v133, v133\n\t" \
        "v_cvt_pk_bf16_f32 v193, v130, v131\n\t" \
        "v_smfmac_f32_16x16x64_bf16 v[72:75], v[176:179], v[184:191], v180\n\t" \
        "v_exp_f32_e32 v134, v134\n\t" \
        "v_exp_f32_e32 v135, v135\n\t" \
        "v_cvt_pk_bf16_f32 v194, v132, v133\n\t" \
        "v_cvt_pk_bf16_f32 v195, v134, v135\n\t" \
        "v_mfma_f32_32x32x16_bf16 v[112:127], v[148:151], v[84:87], v[112:127]\n\t" \
        "v_exp_f32_e32 v136, v136\n\t" \
        "v_exp_f32_e32 v137, v137\n\t" \
        "v_exp_f32_e32 v138, v138\n\t" \
        "v_cvt_pk_bf16_f32 v196, v136, v137\n\t" \
        "v_mfma_f32_32x32x16_bf16 v[48:63], v[168:171], v[192:195], v[48:63]\n\t" \
        "v_exp_f32_e32 v139, v139\n\t" \
        "v_exp_f32_e32 v140, v140\n\t" \
        "v_exp_f32_e32 v141, v141\n\t" \
        "v_cvt_pk_bf16_f32 v197, v138, v139\n\t" \
        "v_exp_f32_e32 v142, v142\n\t" \
        "v_exp_f32_e32 v143, v143\n\t" \
        "v_cvt_pk_bf16_f32 v198, v140, v141\n\t" \
        "v_cvt_pk_bf16_f32 v199, v142, v143\n\t" \
        "v_mfma_f32_32x32x16_bf16 v[128:143], v[144:147], v[88:91], 0\n\t" \
        "v_exp_f32_e32 v112, v112\n\t" \
        "v_exp_f32_e32 v113, v113\n\t" \
        "v_exp_f32_e32 v114, v114\n\t" \
        "v_cvt_pk_bf16_f32 v184, v112, v113\n\t" \
        "v_mfma_f32_32x32x16_bf16 v[48:63], v[172:175], v[196:199], v[48:63]\n\t" \
        "ds_read_b128 v[152:155], v181 offset:20480\n\t" \
        "v_exp_f32_e32 v115, v115\n\t" \
        "v_exp_f32_e32 v116, v116\n\t" \
        "v_exp_f32_e32 v117, v117\n\t" \
        "v_cvt_pk_bf16_f32 v185, v114, v115\n\t" \
        "v_smfmac_f32_16x16x64_bf16 v[76:79], v[176:179], v[192:199], v180\n\t" \
        "v_exp_f32_e32 v118, v118\n\t" \
        "v_exp_f32_e32 v119, v119\n\t" \
        "v_cvt_pk_bf16_f32 v186, v116, v117\n\t" \
        "v_cvt_pk_bf16_f32 v187, v118, v119\n\t" \
        "v_mfma_f32_32x32x16_bf16 v[128:143], v[148:151], v[92:95], v[128:143]\n\t" \
        "v_exp_f32_e32 v120, v120\n\t" \
        "v_exp_f32_e32 v121, v121\n\t" \
        "v_exp_f32_e32 v122, v122\n\t" \
        "v_cvt_pk_bf16_f32 v188, v120, v121\n\t" \
        "v_mfma_f32_32x32x16_bf16 v[0:15], v[160:163], v[184:187], v[0:15]\n\t" \
        "ds_read_b128 v[156:159], v181 offset:21504\n\t" \
        "v_exp_f32_e32 v123, v123\n\t" \
        "v_exp_f32_e32 v124, v124\n\t" \
        "v_exp_f32_e32 v125, v125\n\t" \
        "v_cvt_pk_bf16_f32 v189, v122, v123\n\t" \
        "v_exp_f32_e32 v126, v126\n\t" \
        "v_exp_f32_e32 v127, v127\n\t" \
        "v_cvt_pk_bf16_f32 v190, v124, v125\n\t" \
        "v_cvt_pk_bf16_f32 v191, v126, v127\n\t" \
        "v_mfma_f32_32x32x16_bf16 v[112:127], v[144:147], v[96:99], 0\n\t" \
        "v_exp_f32_e32 v128, v128\n\t" \
        "v_exp_f32_e32 v129, v129\n\t" \
        "v_exp_f32_e32 v130, v130\n\t" \
        "v_cvt_pk_bf16_f32 v192, v128, v129\n\t" \
        "v_mfma_f32_32x32x16_bf16 v[0:15], v[164:167], v[188:191], v[0:15]\n\t" \
        "ds_read_b128 v[168:171], v181 offset:22528\n\t" \
        "v_exp_f32_e32 v131, v131\n\t" \
        "v_exp_f32_e32 v132, v132\n\t" \
        "v_exp_f32_e32 v133, v133\n\t" \
        "v_cvt_pk_bf16_f32 v193, v130, v131\n\t" \
        "v_smfmac_f32_16x16x64_bf16 v[64:67], v[176:179], v[184:191], v180\n\t" \
        "v_exp_f32_e32 v134, v134\n\t" \
        "v_exp_f32_e32 v135, v135\n\t" \
        "v_cvt_pk_bf16_f32 v194, v132, v133\n\t" \
        "v_cvt_pk_bf16_f32 v195, v134, v135\n\t" \
        "v_mfma_f32_32x32x16_bf16 v[112:127], v[148:151], v[100:103], v[112:127]\n\t" \
        "v_exp_f32_e32 v136, v136\n\t" \
        "v_exp_f32_e32 v137, v137\n\t" \
        "v_exp_f32_e32 v138, v138\n\t" \
        "v_cvt_pk_bf16_f32 v196, v136, v137\n\t" \
        "v_mfma_f32_32x32x16_bf16 v[16:31], v[160:163], v[192:195], v[16:31]\n\t" \
        "ds_read_b128 v[172:175], v181 offset:23552\n\t" \
        "v_exp_f32_e32 v139, v139\n\t" \
        "v_exp_f32_e32 v140, v140\n\t" \
        "v_exp_f32_e32 v141, v141\n\t" \
        "v_cvt_pk_bf16_f32 v197, v138, v139\n\t" \
        "v_exp_f32_e32 v142, v142\n\t" \
        "v_exp_f32_e32 v143, v143\n\t" \
        "v_cvt_pk_bf16_f32 v198, v140, v141\n\t" \
        "v_cvt_pk_bf16_f32 v199, v142, v143\n\t" \
        "v_mfma_f32_32x32x16_bf16 v[128:143], v[144:147], v[104:107], 0\n\t" \
        "v_exp_f32_e32 v112, v112\n\t" \
        "v_exp_f32_e32 v113, v113\n\t" \
        "v_exp_f32_e32 v114, v114\n\t" \
        "v_cvt_pk_bf16_f32 v184, v112, v113\n\t" \
        "v_mfma_f32_32x32x16_bf16 v[16:31], v[164:167], v[196:199], v[16:31]\n\t" \
        "v_exp_f32_e32 v115, v115\n\t" \
        "v_exp_f32_e32 v116, v116\n\t" \
        "v_exp_f32_e32 v117, v117\n\t" \
        "v_cvt_pk_bf16_f32 v185, v114, v115\n\t" \
        "v_smfmac_f32_16x16x64_bf16 v[68:71], v[176:179], v[192:199], v180\n\t" \
        "v_exp_f32_e32 v118, v118\n\t" \
        "v_exp_f32_e32 v119, v119\n\t" \
        "v_cvt_pk_bf16_f32 v186, v116, v117\n\t" \
        "v_cvt_pk_bf16_f32 v187, v118, v119\n\t" \
        "v_mfma_f32_32x32x16_bf16 v[128:143], v[148:151], v[108:111], v[128:143]\n\t" \
        "v_exp_f32_e32 v120, v120\n\t" \
        "v_exp_f32_e32 v121, v121\n\t" \
        "v_exp_f32_e32 v122, v122\n\t" \
        "v_cvt_pk_bf16_f32 v188, v120, v121\n\t" \
        "v_mfma_f32_32x32x16_bf16 v[32:47], v[160:163], v[184:187], v[32:47]\n\t" \
        "v_exp_f32_e32 v123, v123\n\t" \
        "v_exp_f32_e32 v124, v124\n\t" \
        "v_exp_f32_e32 v125, v125\n\t" \
        "v_cvt_pk_bf16_f32 v189, v122, v123\n\t" \
        "v_exp_f32_e32 v126, v126\n\t" \
        "v_exp_f32_e32 v127, v127\n\t" \
        "v_cvt_pk_bf16_f32 v190, v124, v125\n\t" \
        "v_cvt_pk_bf16_f32 v191, v126, v127\n\t" \
        "s_waitcnt lgkmcnt(0)\n\t" \
        "v_mfma_f32_32x32x16_bf16 v[112:127], v[152:155], v[80:83], 0\n\t" \
        "v_exp_f32_e32 v128, v128\n\t" \
        "v_exp_f32_e32 v129, v129\n\t" \
        "v_exp_f32_e32 v130, v130\n\t" \
        "v_cvt_pk_bf16_f32 v192, v128, v129\n\t" \
        "v_mfma_f32_32x32x16_bf16 v[32:47], v[164:167], v[188:191], v[32:47]\n\t" \
        "v_exp_f32_e32 v131, v131\n\t" \
        "v_exp_f32_e32 v132, v132\n\t" \
        "v_exp_f32_e32 v133, v133\n\t" \
        "v_cvt_pk_bf16_f32 v193, v130, v131\n\t" \
        "v_smfmac_f32_16x16x64_bf16 v[72:75], v[176:179], v[184:191], v180\n\t" \
        "v_exp_f32_e32 v134, v134\n\t" \
        "v_exp_f32_e32 v135, v135\n\t" \
        "v_cvt_pk_bf16_f32 v194, v132, v133\n\t" \
        "v_cvt_pk_bf16_f32 v195, v134, v135\n\t" \
        "v_mfma_f32_32x32x16_bf16 v[112:127], v[156:159], v[84:87], v[112:127]\n\t" \
        "v_exp_f32_e32 v136, v136\n\t" \
        "v_exp_f32_e32 v137, v137\n\t" \
        "v_exp_f32_e32 v138, v138\n\t" \
        "v_cvt_pk_bf16_f32 v196, v136, v137\n\t" \
        "v_mfma_f32_32x32x16_bf16 v[48:63], v[160:163], v[192:195], v[48:63]\n\t" \
        "v_exp_f32_e32 v139, v139\n\t" \
        "v_exp_f32_e32 v140, v140\n\t" \
        "v_exp_f32_e32 v141, v141\n\t" \
        "v_cvt_pk_bf16_f32 v197, v138, v139\n\t" \
        "v_exp_f32_e32 v142, v142\n\t" \
        "v_exp_f32_e32 v143, v143\n\t" \
        "v_cvt_pk_bf16_f32 v198, v140, v141\n\t" \
        "v_cvt_pk_bf16_f32 v199, v142, v143\n\t" \
        "v_mfma_f32_32x32x16_bf16 v[128:143], v[152:155], v[88:91], 0\n\t" \
        "v_exp_f32_e32 v112, v112\n\t" \
        "v_exp_f32_e32 v113, v113\n\t" \
        "v_exp_f32_e32 v114, v114\n\t" \
        "v_cvt_pk_bf16_f32 v184, v112, v113\n\t" \
        "v_mfma_f32_32x32x16_bf16 v[48:63], v[164:167], v[196:199], v[48:63]\n\t" \
        "ds_read_b128 v[144:147], v181 offset:24576\n\t" \
        "v_exp_f32_e32 v115, v115\n\t" \
        "v_exp_f32_e32 v116, v116\n\t" \
        "v_exp_f32_e32 v117, v117\n\t" \
        "v_cvt_pk_bf16_f32 v185, v114, v115\n\t" \
        "v_smfmac_f32_16x16x64_bf16 v[76:79], v[176:179], v[192:199], v180\n\t" \
        "v_exp_f32_e32 v118, v118\n\t" \
        "v_exp_f32_e32 v119, v119\n\t" \
        "v_cvt_pk_bf16_f32 v186, v116, v117\n\t" \
        "v_cvt_pk_bf16_f32 v187, v118, v119\n\t" \
        "v_mfma_f32_32x32x16_bf16 v[128:143], v[156:159], v[92:95], v[128:143]\n\t" \
        "v_exp_f32_e32 v120, v120\n\t" \
        "v_exp_f32_e32 v121, v121\n\t" \
        "v_exp_f32_e32 v122, v122\n\t" \
        "v_cvt_pk_bf16_f32 v188, v120, v121\n\t" \
        "v_mfma_f32_32x32x16_bf16 v[0:15], v[168:171], v[184:187], v[0:15]\n\t" \
        "ds_read_b128 v[148:151], v181 offset:25600\n\t" \
        "v_exp_f32_e32 v123, v123\n\t" \
        "v_exp_f32_e32 v124, v124\n\t" \
        "v_exp_f32_e32 v125, v125\n\t" \
        "v_cvt_pk_bf16_f32 v189, v122, v123\n\t" \
        "v_exp_f32_e32 v126, v126\n\t" \
        "v_exp_f32_e32 v127, v127\n\t" \
        "v_cvt_pk_bf16_f32 v190, v124, v125\n\t" \
        "v_cvt_pk_bf16_f32 v191, v126, v127\n\t" \
        "v_mfma_f32_32x32x16_bf16 v[112:127], v[152:155], v[96:99], 0\n\t" \
        "v_exp_f32_e32 v128, v128\n\t" \
        "v_exp_f32_e32 v129, v129\n\t" \
        "v_exp_f32_e32 v130, v130\n\t" \
        "v_cvt_pk_bf16_f32 v192, v128, v129\n\t" \
        "v_mfma_f32_32x32x16_bf16 v[0:15], v[172:175], v[188:191], v[0:15]\n\t" \
        "ds_read_b128 v[160:163], v181 offset:26624\n\t" \
        "v_exp_f32_e32 v131, v131\n\t" \
        "v_exp_f32_e32 v132, v132\n\t" \
        "v_exp_f32_e32 v133, v133\n\t" \
        "v_cvt_pk_bf16_f32 v193, v130, v131\n\t" \
        "v_smfmac_f32_16x16x64_bf16 v[64:67], v[176:179], v[184:191], v180\n\t" \
        "v_exp_f32_e32 v134, v134\n\t" \
        "v_exp_f32_e32 v135, v135\n\t" \
        "v_cvt_pk_bf16_f32 v194, v132, v133\n\t" \
        "v_cvt_pk_bf16_f32 v195, v134, v135\n\t" \
        "v_mfma_f32_32x32x16_bf16 v[112:127], v[156:159], v[100:103], v[112:127]\n\t" \
        "v_exp_f32_e32 v136, v136\n\t" \
        "v_exp_f32_e32 v137, v137\n\t" \
        "v_exp_f32_e32 v138, v138\n\t" \
        "v_cvt_pk_bf16_f32 v196, v136, v137\n\t" \
        "v_mfma_f32_32x32x16_bf16 v[16:31], v[168:171], v[192:195], v[16:31]\n\t" \
        "ds_read_b128 v[164:167], v181 offset:27648\n\t" \
        "v_exp_f32_e32 v139, v139\n\t" \
        "v_exp_f32_e32 v140, v140\n\t" \
        "v_exp_f32_e32 v141, v141\n\t" \
        "v_cvt_pk_bf16_f32 v197, v138, v139\n\t" \
        "v_exp_f32_e32 v142, v142\n\t" \
        "v_exp_f32_e32 v143, v143\n\t" \
        "v_cvt_pk_bf16_f32 v198, v140, v141\n\t" \
        "v_cvt_pk_bf16_f32 v199, v142, v143\n\t" \
        "v_mfma_f32_32x32x16_bf16 v[128:143], v[152:155], v[104:107], 0\n\t" \
        "v_exp_f32_e32 v112, v112\n\t" \
        "v_exp_f32_e32 v113, v113\n\t" \
        "v_exp_f32_e32 v114, v114\n\t" \
        "v_cvt_pk_bf16_f32 v184, v112, v113\n\t" \
        "v_mfma_f32_32x32x16_bf16 v[16:31], v[172:175], v[196:199], v[16:31]\n\t" \
        "v_exp_f32_e32 v115, v115\n\t" \
        "v_exp_f32_e32 v116, v116\n\t" \
        "v_exp_f32_e32 v117, v117\n\t" \
        "v_cvt_pk_bf16_f32 v185, v114, v115\n\t" \
        "v_smfmac_f32_16x16x64_bf16 v[68:71], v[176:179], v[192:199], v180\n\t" \
        "v_exp_f32_e32 v118, v118\n\t" \
        "v_exp_f32_e32 v119, v119\n\t" \
        "v_cvt_pk_bf16_f32 v186, v116, v117\n\t" \
        "v_cvt_pk_bf16_f32 v187, v118, v119\n\t" \
        "v_mfma_f32_32x32x16_bf16 v[128:143], v[156:159], v[108:111], v[128:143]\n\t" \
        "v_exp_f32_e32 v120, v120\n\t" \
        "v_exp_f32_e32 v121, v121\n\t" \
        "v_exp_f32_e32 v122, v122\n\t" \
        "v_cvt_pk_bf16_f32 v188, v120, v121\n\t" \
        "v_mfma_f32_32x32x16_bf16 v[32:47], v[168:171], v[184:187], v[32:47]\n\t" \
        "v_exp_f32_e32 v123, v123\n\t" \
        "v_exp_f32_e32 v124, v124\n\t" \
        "v_exp_f32_e32 v125, v125\n\t" \
        "v_cvt_pk_bf16_f32 v189, v122, v123\n\t" \
        "v_exp_f32_e32 v126, v126\n\t" \
        "v_exp_f32_e32 v127, v127\n\t" \
        "v_cvt_pk_bf16_f32 v190, v124, v125\n\t" \
        "v_cvt_pk_bf16_f32 v191, v126, v127\n\t" \
        "s_waitcnt lgkmcnt(0)\n\t" \
        "v_mfma_f32_32x32x16_bf16 v[112:127], v[144:147], v[80:83], 0\n\t" \
        "v_exp_f32_e32 v128, v128\n\t" \
        "v_exp_f32_e32 v129, v129\n\t" \
        "v_exp_f32_e32 v130, v130\n\t" \
        "v_cvt_pk_bf16_f32 v192, v128, v129\n\t" \
        "v_mfma_f32_32x32x16_bf16 v[32:47], v[172:175], v[188:191], v[32:47]\n\t" \
        "v_exp_f32_e32 v131, v131\n\t" \
        "v_exp_f32_e32 v132, v132\n\t" \
        "v_exp_f32_e32 v133, v133\n\t" \
        "v_cvt_pk_bf16_f32 v193, v130, v131\n\t" \
        "v_smfmac_f32_16x16x64_bf16 v[72:75], v[176:179], v[184:191], v180\n\t" \
        "v_exp_f32_e32 v134, v134\n\t" \
        "v_exp_f32_e32 v135, v135\n\t" \
        "v_cvt_pk_bf16_f32 v194, v132, v133\n\t" \
        "v_cvt_pk_bf16_f32 v195, v134, v135\n\t" \
        "v_mfma_f32_32x32x16_bf16 v[112:127], v[148:151], v[84:87], v[112:127]\n\t" \
        "v_exp_f32_e32 v136, v136\n\t" \
        "v_exp_f32_e32 v137, v137\n\t" \
        "v_exp_f32_e32 v138, v138\n\t" \
        "v_cvt_pk_bf16_f32 v196, v136, v137\n\t" \
        "v_mfma_f32_32x32x16_bf16 v[48:63], v[168:171], v[192:195], v[48:63]\n\t" \
        "v_exp_f32_e32 v139, v139\n\t" \
        "v_exp_f32_e32 v140, v140\n\t" \
        "v_exp_f32_e32 v141, v141\n\t" \
        "v_cvt_pk_bf16_f32 v197, v138, v139\n\t" \
        "v_exp_f32_e32 v142, v142\n\t" \
        "v_exp_f32_e32 v143, v143\n\t" \
        "v_cvt_pk_bf16_f32 v198, v140, v141\n\t" \
        "v_cvt_pk_bf16_f32 v199, v142, v143\n\t" \
        "v_mfma_f32_32x32x16_bf16 v[128:143], v[144:147], v[88:91], 0\n\t" \
        "v_exp_f32_e32 v112, v112\n\t" \
        "v_exp_f32_e32 v113, v113\n\t" \
        "v_exp_f32_e32 v114, v114\n\t" \
        "v_cvt_pk_bf16_f32 v184, v112, v113\n\t" \
        "v_mfma_f32_32x32x16_bf16 v[48:63], v[172:175], v[196:199], v[48:63]\n\t" \
        "ds_read_b128 v[152:155], v181 offset:28672\n\t" \
        "v_exp_f32_e32 v115, v115\n\t" \
        "v_exp_f32_e32 v116, v116\n\t" \
        "v_exp_f32_e32 v117, v117\n\t" \
        "v_cvt_pk_bf16_f32 v185, v114, v115\n\t" \
        "v_smfmac_f32_16x16x64_bf16 v[76:79], v[176:179], v[192:199], v180\n\t" \
        "v_exp_f32_e32 v118, v118\n\t" \
        "v_exp_f32_e32 v119, v119\n\t" \
        "v_cvt_pk_bf16_f32 v186, v116, v117\n\t" \
        "v_cvt_pk_bf16_f32 v187, v118, v119\n\t" \
        "v_mfma_f32_32x32x16_bf16 v[128:143], v[148:151], v[92:95], v[128:143]\n\t" \
        "v_exp_f32_e32 v120, v120\n\t" \
        "v_exp_f32_e32 v121, v121\n\t" \
        "v_exp_f32_e32 v122, v122\n\t" \
        "v_cvt_pk_bf16_f32 v188, v120, v121\n\t" \
        "v_mfma_f32_32x32x16_bf16 v[0:15], v[160:163], v[184:187], v[0:15]\n\t" \
        "ds_read_b128 v[156:159], v181 offset:29696\n\t" \
        "v_exp_f32_e32 v123, v123\n\t" \
        "v_exp_f32_e32 v124, v124\n\t" \
        "v_exp_f32_e32 v125, v125\n\t" \
        "v_cvt_pk_bf16_f32 v189, v122, v123\n\t" \
        "v_exp_f32_e32 v126, v126\n\t" \
        "v_exp_f32_e32 v127, v127\n\t" \
        "v_cvt_pk_bf16_f32 v190, v124, v125\n\t" \
        "v_cvt_pk_bf16_f32 v191, v126, v127\n\t" \
        "v_mfma_f32_32x32x16_bf16 v[112:127], v[144:147], v[96:99], 0\n\t" \
        "v_exp_f32_e32 v128, v128\n\t" \
        "v_exp_f32_e32 v129, v129\n\t" \
        "v_exp_f32_e32 v130, v130\n\t" \
        "v_cvt_pk_bf16_f32 v192, v128, v129\n\t" \
        "v_mfma_f32_32x32x16_bf16 v[0:15], v[164:167], v[188:191], v[0:15]\n\t" \
        "ds_read_b128 v[168:171], v181 offset:30720\n\t" \
        "v_exp_f32_e32 v131, v131\n\t" \
        "v_exp_f32_e32 v132, v132\n\t" \
        "v_exp_f32_e32 v133, v133\n\t" \
        "v_cvt_pk_bf16_f32 v193, v130, v131\n\t" \
        "v_smfmac_f32_16x16x64_bf16 v[64:67], v[176:179], v[184:191], v180\n\t" \
        "v_exp_f32_e32 v134, v134\n\t" \
        "v_exp_f32_e32 v135, v135\n\t" \
        "v_cvt_pk_bf16_f32 v194, v132, v133\n\t" \
        "v_cvt_pk_bf16_f32 v195, v134, v135\n\t" \
        "v_mfma_f32_32x32x16_bf16 v[112:127], v[148:151], v[100:103], v[112:127]\n\t" \
        "v_exp_f32_e32 v136, v136\n\t" \
        "v_exp_f32_e32 v137, v137\n\t" \
        "v_exp_f32_e32 v138, v138\n\t" \
        "v_cvt_pk_bf16_f32 v196, v136, v137\n\t" \
        "v_mfma_f32_32x32x16_bf16 v[16:31], v[160:163], v[192:195], v[16:31]\n\t" \
        "ds_read_b128 v[172:175], v181 offset:31744\n\t" \
        "v_exp_f32_e32 v139, v139\n\t" \
        "v_exp_f32_e32 v140, v140\n\t" \
        "v_exp_f32_e32 v141, v141\n\t" \
        "v_cvt_pk_bf16_f32 v197, v138, v139\n\t" \
        "v_exp_f32_e32 v142, v142\n\t" \
        "v_exp_f32_e32 v143, v143\n\t" \
        "v_cvt_pk_bf16_f32 v198, v140, v141\n\t" \
        "v_cvt_pk_bf16_f32 v199, v142, v143\n\t" \
        "v_mfma_f32_32x32x16_bf16 v[128:143], v[144:147], v[104:107], 0\n\t" \
        "v_exp_f32_e32 v112, v112\n\t" \
        "v_exp_f32_e32 v113, v113\n\t" \
        "v_exp_f32_e32 v114, v114\n\t" \
        "v_cvt_pk_bf16_f32 v184, v112, v113\n\t" \
        "v_mfma_f32_32x32x16_bf16 v[16:31], v[164:167], v[196:199], v[16:31]\n\t" \
        "v_exp_f32_e32 v115, v115\n\t" \
        "v_exp_f32_e32 v116, v116\n\t" \
        "v_exp_f32_e32 v117, v117\n\t" \
        "v_cvt_pk_bf16_f32 v185, v114, v115\n\t" \
        "v_smfmac_f32_16x16x64_bf16 v[68:71], v[176:179], v[192:199], v180\n\t" \
        "v_exp_f32_e32 v118, v118\n\t" \
        "v_exp_f32_e32 v119, v119\n\t" \
        "v_cvt_pk_bf16_f32 v186, v116, v117\n\t" \
        "v_cvt_pk_bf16_f32 v187, v118, v119\n\t" \
        "v_mfma_f32_32x32x16_bf16 v[128:143], v[148:151], v[108:111], v[128:143]\n\t" \
        "v_exp_f32_e32 v120, v120\n\t" \
        "v_exp_f32_e32 v121, v121\n\t" \
        "v_exp_f32_e32 v122, v122\n\t" \
        "v_cvt_pk_bf16_f32 v188, v120, v121\n\t" \
        "v_mfma_f32_32x32x16_bf16 v[32:47], v[160:163], v[184:187], v[32:47]\n\t" \
        "v_exp_f32_e32 v123, v123\n\t" \
        "v_exp_f32_e32 v124, v124\n\t" \
        "v_exp_f32_e32 v125, v125\n\t" \
        "v_cvt_pk_bf16_f32 v189, v122, v123\n\t" \
        "v_exp_f32_e32 v126, v126\n\t" \
        "v_exp_f32_e32 v127, v127\n\t" \
        "v_cvt_pk_bf16_f32 v190, v124, v125\n\t" \
        "v_cvt_pk_bf16_f32 v191, v126, v127\n\t" \
        "s_waitcnt lgkmcnt(0)\n\t" \
        "v_mfma_f32_32x32x16_bf16 v[112:127], v[152:155], v[80:83], 0\n\t" \
        "v_exp_f32_e32 v128, v128\n\t" \
        "v_exp_f32_e32 v129, v129\n\t" \
        "v_exp_f32_e32 v130, v130\n\t" \
        "v_cvt_pk_bf16_f32 v192, v128, v129\n\t" \
        "v_mfma_f32_32x32x16_bf16 v[32:47], v[164:167], v[188:191], v[32:47]\n\t" \
        "v_exp_f32_e32 v131, v131\n\t" \
        "v_exp_f32_e32 v132, v132\n\t" \
        "v_exp_f32_e32 v133, v133\n\t" \
        "v_cvt_pk_bf16_f32 v193, v130, v131\n\t" \
        "v_smfmac_f32_16x16x64_bf16 v[72:75], v[176:179], v[184:191], v180\n\t" \
        "v_exp_f32_e32 v134, v134\n\t" \
        "v_exp_f32_e32 v135, v135\n\t" \
        "v_cvt_pk_bf16_f32 v194, v132, v133\n\t" \
        "v_cvt_pk_bf16_f32 v195, v134, v135\n\t" \
        "v_mfma_f32_32x32x16_bf16 v[112:127], v[156:159], v[84:87], v[112:127]\n\t" \
        "v_exp_f32_e32 v136, v136\n\t" \
        "v_exp_f32_e32 v137, v137\n\t" \
        "v_exp_f32_e32 v138, v138\n\t" \
        "v_cvt_pk_bf16_f32 v196, v136, v137\n\t" \
        "v_mfma_f32_32x32x16_bf16 v[48:63], v[160:163], v[192:195], v[48:63]\n\t" \
        "v_exp_f32_e32 v139, v139\n\t" \
        "v_exp_f32_e32 v140, v140\n\t" \
        "v_exp_f32_e32 v141, v141\n\t" \
        "v_cvt_pk_bf16_f32 v197, v138, v139\n\t" \
        "v_exp_f32_e32 v142, v142\n\t" \
        "v_exp_f32_e32 v143, v143\n\t" \
        "v_cvt_pk_bf16_f32 v198, v140, v141\n\t" \
        "v_cvt_pk_bf16_f32 v199, v142, v143\n\t" \
        "s_barrier\n\t" \
        "v_mfma_f32_32x32x16_bf16 v[128:143], v[152:155], v[88:91], 0\n\t" \
        "v_exp_f32_e32 v112, v112\n\t" \
        "v_exp_f32_e32 v113, v113\n\t" \
        "v_exp_f32_e32 v114, v114\n\t" \
        "v_cvt_pk_bf16_f32 v184, v112, v113\n\t" \
        "v_mfma_f32_32x32x16_bf16 v[48:63], v[164:167], v[196:199], v[48:63]\n\t" \
        "ds_read_b128 v[144:147], v182 offset:0\n\t" \
        "v_exp_f32_e32 v115, v115\n\t" \
        "v_exp_f32_e32 v116, v116\n\t" \
        "v_exp_f32_e32 v117, v117\n\t" \
        "v_cvt_pk_bf16_f32 v185, v114, v115\n\t" \
        "v_smfmac_f32_16x16x64_bf16 v[76:79], v[176:179], v[192:199], v180\n\t" \
        "v_exp_f32_e32 v118, v118\n\t" \
        "v_exp_f32_e32 v119, v119\n\t" \
        "v_cvt_pk_bf16_f32 v186, v116, v117\n\t" \
        "v_cvt_pk_bf16_f32 v187, v118, v119\n\t" \
        "v_mfma_f32_32x32x16_bf16 v[128:143], v[156:159], v[92:95], v[128:143]\n\t" \
        "v_exp_f32_e32 v120, v120\n\t" \
        "v_exp_f32_e32 v121, v121\n\t" \
        "v_exp_f32_e32 v122, v122\n\t" \
        "v_cvt_pk_bf16_f32 v188, v120, v121\n\t" \
        "v_mfma_f32_32x32x16_bf16 v[0:15], v[168:171], v[184:187], v[0:15]\n\t" \
        "ds_read_b128 v[148:151], v182 offset:1024\n\t" \
        "v_exp_f32_e32 v123, v123\n\t" \
        "v_exp_f32_e32 v124, v124\n\t" \
        "v_exp_f32_e32 v125, v125\n\t" \
        "v_cvt_pk_bf16_f32 v189, v122, v123\n\t" \
        "v_exp_f32_e32 v126, v126\n\t" \
        "v_exp_f32_e32 v127, v127\n\t" \
        "v_cvt_pk_bf16_f32 v190, v124, v125\n\t" \
        "v_cvt_pk_bf16_f32 v191, v126, v127\n\t" \
        "v_mfma_f32_32x32x16_bf16 v[112:127], v[152:155], v[96:99], 0\n\t" \
        "v_exp_f32_e32 v128, v128\n\t" \
        "v_exp_f32_e32 v129, v129\n\t" \
        "v_exp_f32_e32 v130, v130\n\t" \
        "v_cvt_pk_bf16_f32 v192, v128, v129\n\t" \
        "v_mfma_f32_32x32x16_bf16 v[0:15], v[172:175], v[188:191], v[0:15]\n\t" \
        "ds_read_b128 v[160:163], v182 offset:2048\n\t" \
        "v_exp_f32_e32 v131, v131\n\t" \
        "v_exp_f32_e32 v132, v132\n\t" \
        "v_exp_f32_e32 v133, v133\n\t" \
        "v_cvt_pk_bf16_f32 v193, v130, v131\n\t" \
        "v_smfmac_f32_16x16x64_bf16 v[64:67], v[176:179], v[184:191], v180\n\t" \
        "v_exp_f32_e32 v134, v134\n\t" \
        "v_exp_f32_e32 v135, v135\n\t" \
        "v_cvt_pk_bf16_f32 v194, v132, v133\n\t" \
        "v_cvt_pk_bf16_f32 v195, v134, v135\n\t" \
        "v_mfma_f32_32x32x16_bf16 v[112:127], v[156:159], v[100:103], v[112:127]\n\t" \
        "v_exp_f32_e32 v136, v136\n\t" \
        "v_exp_f32_e32 v137, v137\n\t" \
        "v_exp_f32_e32 v138, v138\n\t" \
        "v_cvt_pk_bf16_f32 v196, v136, v137\n\t" \
        "v_mfma_f32_32x32x16_bf16 v[16:31], v[168:171], v[192:195], v[16:31]\n\t" \
        "ds_read_b128 v[164:167], v182 offset:3072\n\t" \
        "v_exp_f32_e32 v139, v139\n\t" \
        "v_exp_f32_e32 v140, v140\n\t" \
        "v_exp_f32_e32 v141, v141\n\t" \
        "v_cvt_pk_bf16_f32 v197, v138, v139\n\t" \
        "v_exp_f32_e32 v142, v142\n\t" \
        "v_exp_f32_e32 v143, v143\n\t" \
        "v_cvt_pk_bf16_f32 v198, v140, v141\n\t" \
        "v_cvt_pk_bf16_f32 v199, v142, v143\n\t" \
        "v_mfma_f32_32x32x16_bf16 v[128:143], v[152:155], v[104:107], 0\n\t" \
        "v_exp_f32_e32 v112, v112\n\t" \
        "v_exp_f32_e32 v113, v113\n\t" \
        "v_exp_f32_e32 v114, v114\n\t" \
        "v_cvt_pk_bf16_f32 v184, v112, v113\n\t" \
        "v_mfma_f32_32x32x16_bf16 v[16:31], v[172:175], v[196:199], v[16:31]\n\t" \
        "v_exp_f32_e32 v115, v115\n\t" \
        "v_exp_f32_e32 v116, v116\n\t" \
        "v_exp_f32_e32 v117, v117\n\t" \
        "v_cvt_pk_bf16_f32 v185, v114, v115\n\t" \
        "v_smfmac_f32_16x16x64_bf16 v[68:71], v[176:179], v[192:199], v180\n\t" \
        "v_exp_f32_e32 v118, v118\n\t" \
        "v_exp_f32_e32 v119, v119\n\t" \
        "v_cvt_pk_bf16_f32 v186, v116, v117\n\t" \
        "v_cvt_pk_bf16_f32 v187, v118, v119\n\t" \
        "v_mfma_f32_32x32x16_bf16 v[128:143], v[156:159], v[108:111], v[128:143]\n\t" \
        "v_exp_f32_e32 v120, v120\n\t" \
        "v_exp_f32_e32 v121, v121\n\t" \
        "v_exp_f32_e32 v122, v122\n\t" \
        "v_cvt_pk_bf16_f32 v188, v120, v121\n\t" \
        "v_mfma_f32_32x32x16_bf16 v[32:47], v[168:171], v[184:187], v[32:47]\n\t" \
        "v_exp_f32_e32 v123, v123\n\t" \
        "v_exp_f32_e32 v124, v124\n\t" \
        "v_exp_f32_e32 v125, v125\n\t" \
        "v_cvt_pk_bf16_f32 v189, v122, v123\n\t" \
        "v_exp_f32_e32 v126, v126\n\t" \
        "v_exp_f32_e32 v127, v127\n\t" \
        "v_cvt_pk_bf16_f32 v190, v124, v125\n\t" \
        "v_cvt_pk_bf16_f32 v191, v126, v127\n\t" \
        "s_waitcnt lgkmcnt(0)\n\t" \
        "v_mfma_f32_32x32x16_bf16 v[112:127], v[144:147], v[80:83], 0\n\t" \
        "v_exp_f32_e32 v128, v128\n\t" \
        "v_exp_f32_e32 v129, v129\n\t" \
        "v_exp_f32_e32 v130, v130\n\t" \
        "v_cvt_pk_bf16_f32 v192, v128, v129\n\t" \
        "v_mfma_f32_32x32x16_bf16 v[32:47], v[172:175], v[188:191], v[32:47]\n\t" \
        "v_exp_f32_e32 v131, v131\n\t" \
        "v_exp_f32_e32 v132, v132\n\t" \
        "v_exp_f32_e32 v133, v133\n\t" \
        "v_cvt_pk_bf16_f32 v193, v130, v131\n\t" \
        "v_smfmac_f32_16x16x64_bf16 v[72:75], v[176:179], v[184:191], v180\n\t" \
        "v_exp_f32_e32 v134, v134\n\t" \
        "v_exp_f32_e32 v135, v135\n\t" \
        "v_cvt_pk_bf16_f32 v194, v132, v133\n\t" \
        "v_cvt_pk_bf16_f32 v195, v134, v135\n\t" \
        "v_mfma_f32_32x32x16_bf16 v[112:127], v[148:151], v[84:87], v[112:127]\n\t" \
        "v_exp_f32_e32 v136, v136\n\t" \
        "v_exp_f32_e32 v137, v137\n\t" \
        "v_exp_f32_e32 v138, v138\n\t" \
        "v_cvt_pk_bf16_f32 v196, v136, v137\n\t" \
        "v_mfma_f32_32x32x16_bf16 v[48:63], v[168:171], v[192:195], v[48:63]\n\t" \
        "v_exp_f32_e32 v139, v139\n\t" \
        "v_exp_f32_e32 v140, v140\n\t" \
        "v_exp_f32_e32 v141, v141\n\t" \
        "v_cvt_pk_bf16_f32 v197, v138, v139\n\t" \
        "v_exp_f32_e32 v142, v142\n\t" \
        "v_exp_f32_e32 v143, v143\n\t" \
        "v_cvt_pk_bf16_f32 v198, v140, v141\n\t" \
        "v_cvt_pk_bf16_f32 v199, v142, v143\n\t" \
        "v_swap_b32 v181, v182\n\t" \
        "s_add_u32 s42, s42, 1\n\t" \
        "s_cmp_lt_u32 s42, 8\n\t" \
        "s_cbranch_scc1 1b\n\t" \
        "v_mfma_f32_32x32x16_bf16 v[48:63], v[172:175], v[196:199], v[48:63]\n\t" \
        "v_smfmac_f32_16x16x64_bf16 v[76:79], v[176:179], v[192:199], v180\n\t" \
        "s_nop 7\n\t" \
        "s_nop 7\n\t"
#define ATTN_LOOP_CLOBBERS "v112", "v113", "v114", "v115", "v116", "v117", "v118", "v119", "v120", "v121", "v122", "v123", "v124", "v125", "v126", "v127", "v128", "v129", "v130", "v131", "v132", "v133", "v134", "v135", "v136", "v137", "v138", "v139", "v140", "v141", "v142", "v143", "v144", "v145", "v146", "v147", "v148", "v149", "v150", "v151", "v152", "v153", "v154", "v155", "v156", "v157", "v158", "v159", "v160", "v161", "v162", "v163", "v164", "v165", "v166", "v167", "v184", "v185", "v186", "v187", "v188", "v189", "v190", "v191"
#define ATTN_ZERO_ASM \
        "v_mov_b32_e32 v0, 0\n\t" \
        "v_mov_b32_e32 v1, 0\n\t" \
        "v_mov_b32_e32 v2, 0\n\t" \
        "v_mov_b32_e32 v3, 0\n\t" \
        "v_mov_b32_e32 v4, 0\n\t" \
        "v_mov_b32_e32 v5, 0\n\t" \
        "v_mov_b32_e32 v6, 0\n\t" \
        "v_mov_b32_e32 v7, 0\n\t" \
        "v_mov_b32_e32 v8, 0\n\t" \
        "v_mov_b32_e32 v9, 0\n\t" \
        "v_mov_b32_e32 v10, 0\n\t" \
        "v_mov_b32_e32 v11, 0\n\t" \
        "v_mov_b32_e32 v12, 0\n\t" \
        "v_mov_b32_e32 v13, 0\n\t" \
        "v_mov_b32_e32 v14, 0\n\t" \
        "v_mov_b32_e32 v15, 0\n\t" \
        "v_mov_b32_e32 v16, 0\n\t" \
        "v_mov_b32_e32 v17, 0\n\t" \
        "v_mov_b32_e32 v18, 0\n\t" \
        "v_mov_b32_e32 v19, 0\n\t" \
        "v_mov_b32_e32 v20, 0\n\t" \
        "v_mov_b32_e32 v21, 0\n\t" \
        "v_mov_b32_e32 v22, 0\n\t" \
        "v_mov_b32_e32 v23, 0\n\t" \
        "v_mov_b32_e32 v24, 0\n\t" \
        "v_mov_b32_e32 v25, 0\n\t" \
        "v_mov_b32_e32 v26, 0\n\t" \
        "v_mov_b32_e32 v27, 0\n\t" \
        "v_mov_b32_e32 v28, 0\n\t" \
        "v_mov_b32_e32 v29, 0\n\t" \
        "v_mov_b32_e32 v30, 0\n\t" \
        "v_mov_b32_e32 v31, 0\n\t" \
        "v_mov_b32_e32 v32, 0\n\t" \
        "v_mov_b32_e32 v33, 0\n\t" \
        "v_mov_b32_e32 v34, 0\n\t" \
        "v_mov_b32_e32 v35, 0\n\t" \
        "v_mov_b32_e32 v36, 0\n\t" \
        "v_mov_b32_e32 v37, 0\n\t" \
        "v_mov_b32_e32 v38, 0\n\t" \
        "v_mov_b32_e32 v39, 0\n\t" \
        "v_mov_b32_e32 v40, 0\n\t" \
        "v_mov_b32_e32 v41, 0\n\t" \
        "v_mov_b32_e32 v42, 0\n\t" \
        "v_mov_b32_e32 v43, 0\n\t" \
        "v_mov_b32_e32 v44, 0\n\t" \
        "v_mov_b32_e32 v45, 0\n\t" \
        "v_mov_b32_e32 v46, 0\n\t" \
        "v_mov_b32_e32 v47, 0\n\t" \
        "v_mov_b32_e32 v48, 0\n\t" \
        "v_mov_b32_e32 v49, 0\n\t" \
        "v_mov_b32_e32 v50, 0\n\t" \
        "v_mov_b32_e32 v51, 0\n\t" \
        "v_mov_b32_e32 v52, 0\n\t" \
        "v_mov_b32_e32 v53, 0\n\t" \
        "v_mov_b32_e32 v54, 0\n\t" \
        "v_mov_b32_e32 v55, 0\n\t" \
        "v_mov_b32_e32 v56, 0\n\t" \
        "v_mov_b32_e32 v57, 0\n\t" \
        "v_mov_b32_e32 v58, 0\n\t" \
        "v_mov_b32_e32 v59, 0\n\t" \
        "v_mov_b32_e32 v60, 0\n\t" \
        "v_mov_b32_e32 v61, 0\n\t" \
        "v_mov_b32_e32 v62, 0\n\t" \
        "v_mov_b32_e32 v63, 0\n\t" \
        "v_mov_b32_e32 v64, 0\n\t" \
        "v_mov_b32_e32 v65, 0\n\t" \
        "v_mov_b32_e32 v66, 0\n\t" \
        "v_mov_b32_e32 v67, 0\n\t" \
        "v_mov_b32_e32 v68, 0\n\t" \
        "v_mov_b32_e32 v69, 0\n\t" \
        "v_mov_b32_e32 v70, 0\n\t" \
        "v_mov_b32_e32 v71, 0\n\t" \
        "v_mov_b32_e32 v72, 0\n\t" \
        "v_mov_b32_e32 v73, 0\n\t" \
        "v_mov_b32_e32 v74, 0\n\t" \
        "v_mov_b32_e32 v75, 0\n\t" \
        "v_mov_b32_e32 v76, 0\n\t" \
        "v_mov_b32_e32 v77, 0\n\t" \
        "v_mov_b32_e32 v78, 0\n\t" \
        "v_mov_b32_e32 v79, 0\n\t" \
        "v_mov_b32_e32 v192, 0\n\t" \
        "v_mov_b32_e32 v193, 0\n\t" \
        "v_mov_b32_e32 v194, 0\n\t" \
        "v_mov_b32_e32 v195, 0\n\t" \
        "v_mov_b32_e32 v196, 0\n\t" \
        "v_mov_b32_e32 v197, 0\n\t" \
        "v_mov_b32_e32 v198, 0\n\t" \
        "v_mov_b32_e32 v199, 0\n\t" \
        "v_mov_b32_e32 v168, 0\n\t" \
        "v_mov_b32_e32 v169, 0\n\t" \
        "v_mov_b32_e32 v170, 0\n\t" \
        "v_mov_b32_e32 v171, 0\n\t" \
        "v_mov_b32_e32 v172, 0\n\t" \
        "v_mov_b32_e32 v173, 0\n\t" \
        "v_mov_b32_e32 v174, 0\n\t" \
        "v_mov_b32_e32 v175, 0\n\t"
#define ATTN_PZERO 192
#define ATTN_USE_NEGM 0
#define ATTN_SMF 1
constexpr int AT_SLOT = 16384, AT_DUMMY = 4 * AT_SLOT, AT_LDS = AT_DUMMY + 256;

DI float half_max(float m) { auto rr = __builtin_amdgcn_permlane32_swap(__float_as_uint(m), __float_as_uint(m), false, false); return fmaxf(__uint_as_float(rr[0]), __uint_as_float(rr[1])); }
DI float max16(const f32x16& s) {
    float a = fmaxf(fmaxf(s[0], s[1]), s[2]), b = fmaxf(fmaxf(s[3], s[4]), s[5]);
#pragma unroll
    for (int i = 6; i < 14; i += 4) { a = fmaxf(fmaxf(a, s[i]), s[i + 1]); b = fmaxf(fmaxf(b, s[i + 2]), s[i + 3]); }
    return fmaxf(fmaxf(a, s[14]), fmaxf(b, s[15]));
}
DI void attn_tile_safe(const char* qp, const char* kv, char* op, int lane) {
    const bf16x8 q0 = *(const bf16x8*)(qp + lane * 16), q1 = *(const bf16x8*)(qp + 1024 + lane * 16);
    f32x16 o = f32x16{}; float m = -INFINITY, l = 0.f;
    for (int kb = 0; kb < 64; ++kb) {
        const char* p = kv + kb * 4096 + lane * 16;
        const bf16x8 k0 = *(const bf16x8*)p, k1 = *(const bf16x8*)(p + 1024), v0 = *(const bf16x8*)(p + 2048), v1 = *(const bf16x8*)(p + 3072);
        f32x16 s = MFMA32(k0, q0, f32x16{}); s = MFMA32(k1, q1, s);
        const float mn = fmaxf(m, half_max(max16(s)));
        const float alpha = __builtin_amdgcn_exp2f(m - mn); m = mn;
        float ls = 0.f;
#pragma unroll
        for (int i = 0; i < 16; ++i) s[i] = __builtin_amdgcn_exp2f(s[i] - mn);
        const bf16x8 p0 = pack8(s, 0), p1 = pack8(s, 8);
#pragma unroll
        for (int i = 0; i < 8; ++i) ls += (float)p0[i] + (float)p1[i];
        l = l * alpha + ls;
        o = o * alpha;
        o = MFMA32(v0, p0, o); o = MFMA32(v1, p1, o);
    }
    auto rr = __builtin_amdgcn_permlane32_swap(__float_as_uint(l), __float_as_uint(l), false, false);
    const float inv = 1.0f / (__uint_as_float(rr[0]) + __uint_as_float(rr[1]));
    o = o * inv;
    const int ooff = ((lane >> 4) & 1) * 32768 + ((lane & 15) + 16 * (lane >> 5)) * 16;
    *(bf16x8*)(op + ooff) = pack8(o, 0);
    *(bf16x8*)(op + ooff + 512) = pack8(o, 8);
}

extern "C" __global__ __launch_bounds__(320, 1) void mqa_mha_attn_kernel(char* __restrict__ ws, const char* __restrict__ woA, const char* __restrict__ woB) {
    extern __shared__ __attribute__((aligned(1024))) char smem[];
    const int tid = threadIdx.x, lane = tid & 63, w = __builtin_amdgcn_readfirstlane(tid >> 6);
    const int b = blockIdx.x, xcd = b & 7, idx = b >> 3;
    const int hs = xcd * 8 + (idx >> 2);
    const char* kv = ws + WS_KV + (size_t)(hs < 32 ? 32 : hs - 32) * 262144;
    const unsigned lds0 = (unsigned)(uintptr_t)smem;
    if (w == 4) {
        const unsigned voff = lane * 16;
        auto stage = [&](int t) {
            const char* src = kv + t * AT_SLOT; const unsigned l = lds0 + (t & 3) * AT_SLOT;
#pragma unroll
            for (int u = 0; u < 4; ++u) { glds16<0>(voff, src + u * 4096, l + u * 4096); glds16<1024>(voff, src + u * 4096, l + u * 4096); glds16<2048>(voff, src + u * 4096, l + u * 4096); glds16<3072>(voff, src + u * 4096, l + u * 4096); }
        };
        stage(1);
        asm volatile("s_barrier" ::: "memory");
        stage(2);
#pragma unroll 1
        for (int t = 0; t < 16; ++t) {
            int tt = t; asm volatile("" : "+s"(tt));
            if (tt < 14) asm volatile("s_waitcnt vmcnt(16)\n\ts_barrier" ::: "memory"); else asm volatile("s_waitcnt vmcnt(0)\n\ts_barrier" ::: "memory");
            if (tt < 13) stage(t + 3);
            if (tt == 4) {
                const char* p = ((xcd >> 2) ? woB : woA) + ((xcd >> 1) & 1) * 1024 + (size_t)(idx * 32 + (lane >> 4)) * 2048 + (lane & 15) * 64;
#pragma unroll
                for (int u = 0; u < 8; ++u) touch64(p + (size_t)u * 4 * 2048, lds0 + AT_DUMMY);
            }
            if (tt == 12 && idx < 2) { const char* code; CODE_ADDR(code, mqa_mha_oproj_kernel); touch64(code + (idx * 64 + lane) * 64, lds0 + AT_DUMMY); }
        }
        asm volatile("s_waitcnt vmcnt(0)" ::: "memory");
        return;
    }
    const int tb0 = (idx & 3) * 16 + w * 4;
    const char* qp = ws + WS_QF + (size_t)hs * 131072 + tb0 * 2048;
    char* op = ws + WS_OF + (size_t)(hs >> 5) * 4194304 + (size_t)tb0 * 65536 + (hs & 31) * 1024;
    { const char* src = kv + w * 4096; const unsigned l = lds0 + w * 4096, voff = lane * 16; glds16<0>(voff, src, l); glds16<1024>(voff, src, l); glds16<2048>(voff, src, l); glds16<3072>(voff, src, l); }
    bf16x8 q[4][2];
#pragma unroll
    for (int t = 0; t < 4; ++t) { q[t][0] = *(const bf16x8*)(qp + t * 2048 + lane * 16); q[t][1] = *(const bf16x8*)(qp + t * 2048 + 1024 + lane * 16); }
    const int row = lane & 15;
    const unsigned onelo = ((row & 6) == 0) ? 0x3f803f80u : 0u, onehi = ((row & 6) == 4) ? 0x3f803f80u : 0u;
    const unsigned idxv = (row & 1) ? 0xEEEEEEEEu : 0x44444444u;
    const bf16x8 ones = __builtin_bit_cast(bf16x8, (u32x4){onelo, onelo, onehi, onehi});
    f32x16 o[4], l; f32x4 pz0, pz1, vz0, vz1;
    asm volatile(ATTN_ZERO_ASM : "={v[0:15]}"(o[0]), "={v[16:31]}"(o[1]), "={v[32:47]}"(o[2]), "={v[48:63]}"(o[3]), "={v[64:79]}"(l),
                 "={v[192:195]}"(pz0), "={v[196:199]}"(pz1), "={v[168:171]}"(vz0), "={v[172:175]}"(vz1));
    asm volatile("s_waitcnt vmcnt(0)\n\ts_barrier" ::: "memory");
    {
        unsigned vcur = lds0 + lane * 16, vnxt = vcur + 2 * AT_SLOT, st = 0;
        typedef __attribute__((ext_vector_type(16))) __bf16 bf16x16; typedef __attribute__((ext_vector_type(32))) __bf16 bf16x32;
        const bf16x16 q0 = __builtin_shufflevector(q[0][0], q[0][1], 0,1,2,3,4,5,6,7,8,9,10,11,12,13,14,15), q1 = __builtin_shufflevector(q[1][0], q[1][1], 0,1,2,3,4,5,6,7,8,9,10,11,12,13,14,15);
        const bf16x16 q2 = __builtin_shufflevector(q[2][0], q[2][1], 0,1,2,3,4,5,6,7,8,9,10,11,12,13,14,15), q3 = __builtin_shufflevector(q[3][0], q[3][1], 0,1,2,3,4,5,6,7,8,9,10,11,12,13,14,15);
        const f32x16 qa = __builtin_bit_cast(f32x16, (bf16x32)__builtin_shufflevector(q0, q1, 0,1,2,3,4,5,6,7,8,9,10,11,12,13,14,15,16,17,18,19,20,21,22,23,24,25,26,27,28,29,30,31));
        const f32x16 qb = __builtin_bit_cast(f32x16, (bf16x32)__builtin_shufflevector(q2, q3, 0,1,2,3,4,5,6,7,8,9,10,11,12,13,14,15,16,17,18,19,20,21,22,23,24,25,26,27,28,29,30,31));
        asm volatile(ATTN_LOOP_ASM
            : "+{v[0:15]}"(o[0]), "+{v[16:31]}"(o[1]), "+{v[32:47]}"(o[2]), "+{v[48:63]}"(o[3]), "+{v[64:79]}"(l),
              "+{v181}"(vcur), "+{v182}"(vnxt), "+{s42}"(st), "+{v[192:195]}"(pz0), "+{v[196:199]}"(pz1), "+{v[168:171]}"(vz0), "+{v[172:175]}"(vz1)
            : "{v[80:95]}"(qa), "{v[96:111]}"(qb), "{v[176:179]}"(ones), "{v180}"(idxv)
            : ATTN_LOOP_CLOBBERS, "scc", "memory");
    }
    const int ooff = ((lane >> 4) & 1) * 32768 + ((lane & 15) + 16 * (lane >> 5)) * 16;
    unsigned redo = 0;
#pragma unroll
    for (int t = 0; t < 4; ++t) {
        const float lsum = l[4 * t] + l[4 * t + 1];
        unsigned omax = 0;
#pragma unroll
        for (int i = 0; i < 16; ++i) omax = max(omax, __float_as_uint(o[t][i]) & 0x7fffffffu);
        const bool ok = (lsum > 0x1p-64f) && (lsum < 0x1p100f) && (omax < 0x7b800000u  );
        if (__builtin_expect(__all(ok), 1)) {
            const f32x16 on = o[t] * (1.0f / lsum);
            st16<ST1>(op + t * 65536 + ooff, pack8(on, 0));
            st16<ST1>(op + t * 65536 + ooff + 512, pack8(on, 8));
        } else {
            redo |= 1u << t;
        }
    }
    if (__builtin_expect(redo != 0, 0)) {
        for (int t = 0; t < 4; ++t)
            if ((redo >> t) & 1) attn_tile_safe(qp + t * 2048, kv, op + t * 65536, lane);
    }
}

constexpr int P3_SLOT = 32768, P3_LDS = 4 * P3_SLOT;

extern "C" __global__ __launch_bounds__(512) void mqa_mha_oproj_kernel(const char* __restrict__ ws, const char* __restrict__ woA, const char* __restrict__ woB, char* __restrict__ out) {
    extern __shared__ __attribute__((aligned(1024))) char smem[];
    const int tid = threadIdx.x, lane = tid & 63, w = __builtin_amdgcn_readfirstlane(tid >> 6);
    const int b = blockIdx.x, xcd = b & 7, idx = b >> 3;
    const int br = xcd >> 2, rb = (xcd & 1) * 8 + (idx & 7), ct = ((xcd >> 1) & 1) * 4 + (idx >> 3);
    const int c16 = lane & 15, g = lane >> 4;
    const unsigned lds0 = (unsigned)(uintptr_t)smem;
    if (__builtin_expect(w >= 4, 1)) {
        const int lw = w - 4;
        const char* og = ws + WS_OF + (size_t)br * 4194304 + (size_t)(rb * 8 + 2 * lw) * 32768;
        const char* wg = (br ? woB : woA) + ct * 256 + (size_t)(16 * lw) * 2048;
        const unsigned oo = (unsigned)(lane * 16);
        const unsigned wo0 = (unsigned)((lane >> 4) * 2048 + (((lane & 15) ^ ((lane >> 4) << 1)) << 4));
        const unsigned wo1 = (unsigned)((4 + (lane >> 4)) * 2048 + (((lane & 15) ^ ((4 + (lane >> 4)) << 1)) << 4));
        auto stage = [&](int t) {
            const unsigned l = lds0 + (t & 3) * P3_SLOT;
            const char* os = og + t * 2048; const char* wsrc = wg + (size_t)t * 64 * 2048;
            glds16<0>(oo, os, l + lw * 4096); glds16<1024>(oo, os, l + lw * 4096);
            glds16<0>(oo, os + 32768, l + lw * 4096 + 2048); glds16<1024>(oo, os + 32768, l + lw * 4096 + 2048);
            glds16<0>(wo0, wsrc, l + 16384 + lw * 4096); glds16<0>(wo1, wsrc, l + 16384 + lw * 4096 + 1024);
            glds16<0>(wo0, wsrc + 8 * 2048, l + 16384 + lw * 4096 + 2048); glds16<0>(wo1, wsrc + 8 * 2048, l + 16384 + lw * 4096 + 3072);
        };
        stage(0);
        asm volatile("s_waitcnt vmcnt(0)\n\ts_barrier" ::: "memory");
        stage(1); stage(2);
#pragma unroll 1
        for (int t = 0; t < 13; ++t) { stage(t + 3); WAIT_BAR(16); }
        WAIT_BAR(8); WAIT_BAR(0); WAIT_BAR(0);
        return;
    }
    const int wm = w >> 1, wn = w & 1, q4 = c16 >> 2, p4 = lane & 3;
    f32x16 acc[4];
    {
        unsigned xa = lds0 + (4 * wm) * 2048 + lane * 16;
        unsigned wa[4];
#pragma unroll
        for (int j = 0; j < 4; ++j) wa[j] = lds0 + (16 * (g >> 1) + 4 * (g & 1) + q4) * 256 + (((4 * wn + j) ^ (4 * (g & 1) + q4)) << 5) + p4 * 8;
#pragma unroll
        for (int i = 0; i < 4; ++i) acc[i] = f32x16{};
        asm volatile("" : "+{v[0:15]}"(acc[0]), "+{v[16:31]}"(acc[1]), "+{v[32:47]}"(acc[2]), "+{v[48:63]}"(acc[3]));
        asm volatile("s_barrier" ::: "memory");
        asm volatile(OPROJ_ASM
            : "+{v[0:15]}"(acc[0]), "+{v[16:31]}"(acc[1]), "+{v[32:47]}"(acc[2]), "+{v[48:63]}"(acc[3]),
              "+{v128}"(xa), "+{v129}"(wa[0]), "+{v130}"(wa[1]), "+{v131}"(wa[2]), "+{v132}"(wa[3]) :
            : OPROJ_CLOBBERS, "scc", "memory");
    }
    char* od = out + (size_t)(rb * 128 + 64 * wm + c16) * 4096 + (br * 1024 + ct * 128 + 64 * wn + 16 * (g & 1) + 8 * (g >> 1)) * 2;
#pragma unroll
    for (int i = 0; i < 4; ++i)
#pragma unroll
        for (int jp = 0; jp < 2; ++jp) {
            const f32x4 a = sub4(acc[i], 2 * jp), c = sub4(acc[i], 2 * jp + 1);
            const auto r0 = __builtin_amdgcn_permlane16_swap(cvtpk(a[0], a[1]), cvtpk(c[0], c[1]), false, false);
            const auto r1 = __builtin_amdgcn_permlane16_swap(cvtpk(a[2], a[3]), cvtpk(c[2], c[3]), false, false);
            st16<ST2>(od + i * 16 * 4096 + jp * 64, (u32x4){r0[0], r1[0], r0[1], r1[1]});
        }
}

extern "C" void kernel_launch(void* const* d_in, const int* in_sizes, int n_in, void* d_out, int out_size, void* d_ws, size_t ws_size, hipStream_t stream) {
    (void)in_sizes; (void)n_in; (void)out_size; (void)ws_size;
    const char* xA = (const char*)d_in[0]; const char* wq = (const char*)d_in[1]; const char* wk = (const char*)d_in[2]; const char* wv = (const char*)d_in[3];
    const char* woA = (const char*)d_in[4]; const char* xB = (const char*)d_in[5]; const char* wqkv = (const char*)d_in[6]; const char* woB = (const char*)d_in[7];
    char* ws = (char*)d_ws;
    (void)hipFuncSetAttribute((const void*)mqa_mha_proj_kernel, hipFuncAttributeMaxDynamicSharedMemorySize, P1_LDS);
    (void)hipFuncSetAttribute((const void*)mqa_mha_attn_kernel, hipFuncAttributeMaxDynamicSharedMemorySize, AT_LDS);
    (void)hipFuncSetAttribute((const void*)mqa_mha_oproj_kernel, hipFuncAttributeMaxDynamicSharedMemorySize, P3_LDS);
    mqa_mha_proj_kernel<<<256, 512, P1_LDS, stream>>>(xA, wq, wk, wv, xB, wqkv, ws);
    mqa_mha_attn_kernel<<<256, 320, AT_LDS, stream>>>(ws, woA, woB);
    mqa_mha_oproj_kernel<<<256, 512, P3_LDS, stream>>>(ws, woA, woB, (char*)d_out);
}
```
